```python
import torch

def _softmax(x, axis=-1):
    return torch.softmax(x, dim=axis)
def _gelu(x):
    return torch.nn.functional.gelu(x, approximate="tanh")
def _silu(x):
    return torch.nn.functional.silu(x)
def _sigmoid(x):
    return torch.sigmoid(x)
def _layernorm(x, axis=-1, eps=1e-5):
    m = x.mean(dim=axis, keepdim=True)
    v = x.var(dim=axis, keepdim=True, unbiased=False)
    return (x - m) / torch.sqrt(v + eps)
def _scatter_add(tgt, idx, src, axis=0):
    return tgt.clone().index_add_(axis, idx, src)
def _segment_sum(src, seg, n):
    out = torch.zeros((n,) + src.shape[1:], dtype=src.dtype)
    return out.index_add_(0, seg, src)
def _pad(x, pw):
    flat = tuple(v for pair in reversed(pw) for v in pair)
    return torch.nn.functional.pad(x, flat)

kernel_name = 'sequential_preLN__softmax_full_mha__mixer_s_mlp__prompt'

def setup_inputs():

    _w = (lambda a: (lambda t: t.to(torch.bfloat16) if t.is_floating_point() else t)(torch.as_tensor(a)))
    def _A_setup():
        B, S, D = 1, 2048, 512
        return {
            "x": _w((lambda *s: torch.randn(*s, dtype=torch.bfloat16) * 0.05)(B, S, D)),
            "wqkv": _w((lambda *s: torch.randn(*s, dtype=torch.bfloat16) * 0.05)(D, 3 * D)),
            "wo": _w((lambda *s: torch.randn(*s, dtype=torch.bfloat16) * 0.05)(D, D)),
        }
    _A_in = _A_setup()

    def _B_setup():
        B, S, D, F = 1, 2048, 512, 1376
        return {
            "x":  _w((lambda *s: torch.randn(*s, dtype=torch.bfloat16) * 0.05)(B, S, D)),
            "w1": _w((lambda *s: torch.randn(*s, dtype=torch.bfloat16) * 3.2)(D, D) * (D ** -0.5)),
            "w2": _w((lambda *s: torch.randn(*s, dtype=torch.bfloat16) * 3.2)(D, D) * (D ** -0.5)),
            "w3": _w((lambda *s: torch.randn(*s, dtype=torch.bfloat16) * 3.2)(D, F) * (D ** -0.5)),
            "w4": _w((lambda *s: torch.randn(*s, dtype=torch.bfloat16) * 3.2)(F, D) * (F ** -0.5)),
        }
    _B_in = _B_setup()

    return {
        "A_x": _A_in["x"],
        "A_wqkv": _A_in["wqkv"],
        "A_wo": _A_in["wo"],
        "B_x": _B_in["x"],
        "B_w1": _B_in["w1"],
        "B_w2": _B_in["w2"],
        "B_w3": _B_in["w3"],
        "B_w4": _B_in["w4"],
    }

def reference(A_x, A_wqkv, A_wo, B_x, B_w1, B_w2, B_w3, B_w4):
    def _A_ref(x, wqkv, wo):
        B, S, D = x.shape
        H = 32; Dh = D // H
        qkv = (lambda x, *d: x.permute(*d))((x @ wqkv).reshape(B, S, 3, H, Dh), 2, 0, 3, 1, 4)
        q, k, v = qkv[0], qkv[1], qkv[2]
        scale = Dh ** -0.5
        scores = torch.einsum("bhqd,bhkd->bhqk", q, k) * scale
        p = _softmax(scores, axis=-1)
        o = torch.einsum("bhqk,bhkd->bhqd", p, v)
        return (lambda x, *d: x.permute(*d))(o, 0, 2, 1, 3).reshape(B, S, D) @ wo

    def _B_ref(x, w1, w2, w3, w4):
        B, S, D = x.shape
        h = _gelu(_layernorm(x, axis=-1) @ w1)
        c = D // 4
        g0 = _pad(h[:, :, 0*c:1*c], ((0, 0), (1, 0), (0, 0)))[:, :S, :]
        g1 = _pad(h[:, :, 1*c:2*c], ((0, 0), (0, 1), (0, 0)))[:, 1:, :]
        g2 = _pad(h[:, :, 2*c:3*c], ((0, 0), (2, 0), (0, 0)))[:, :S, :]
        g3 = _pad(h[:, :, 3*c:   ], ((0, 0), (0, 2), (0, 0)))[:, 2:, :]
        hs = torch.concatenate([g0, g1, g2, g3], axis=-1) @ w2
        x1 = x + hs
        h2 = _gelu(_layernorm(x1, axis=-1) @ w3) @ w4
        return x1 + h2

    x = A_x
    h = x + _A_ref(_layernorm(x, axis=-1), A_wqkv, A_wo)
    h2 = h + _B_ref(_layernorm(h, axis=-1), B_w1, B_w2, B_w3, B_w4)
    return h2
```

```cpp
#include <hip/hip_runtime.h>
#include <hip/hip_bf16.h>
#include <stdint.h>

typedef __attribute__((ext_vector_type(8))) short bf16x8;
typedef __attribute__((ext_vector_type(4))) short s16x4;
typedef __attribute__((ext_vector_type(16))) float f32x16;
typedef __attribute__((ext_vector_type(4))) float f32x4;
typedef __attribute__((ext_vector_type(2))) float f32x2;
typedef __attribute__((ext_vector_type(2))) __bf16 bf16x2_t;
typedef __attribute__((ext_vector_type(4))) unsigned u32x4;
typedef __attribute__((ext_vector_type(2))) unsigned u32x2;
typedef unsigned short u16;

#define DI __device__ __forceinline__
#define LDSP __attribute__((address_space(3)))
#define MFMA32(a, b, c) __builtin_amdgcn_mfma_f32_32x32x16_bf16((a), (b), (c), 0, 0, 0)

constexpr int S = 2048, D = 512, NH = 32, F = 1376, FP = 1408;
constexpr float LN_EPS = 1e-5f;
constexpr float QSCALE = 0.25f * 1.4426950408889634f;

DI unsigned pk_bf16(float a, float b) {
  f32x2 v = {a, b};
  bf16x2_t r = __builtin_convertvector(v, bf16x2_t);
  return __builtin_bit_cast(unsigned, r);
}
DI float bf_lo(unsigned u) { return __uint_as_float(u << 16); }
DI float bf_hi(unsigned u) { return __uint_as_float(u & 0xffff0000u); }
DI float rbf(float x) { return bf_lo(pk_bf16(x, x)); }

template <int CTRL> DI float dpp_add(float v) {
  int r = __builtin_amdgcn_update_dpp(0, __float_as_int(v), CTRL, 0xf, 0xf, true);
  return v + __int_as_float(r);
}
DI float wave_sum(float v) {
  v = dpp_add<0xB1>(v);
  v = dpp_add<0x4E>(v);
  v = dpp_add<0x141>(v);
  v = dpp_add<0x140>(v);
  auto a = __builtin_amdgcn_permlane16_swap(__float_as_uint(v), __float_as_uint(v), false, false);
  v = __uint_as_float(a[0]) + __uint_as_float(a[1]);
  auto b = __builtin_amdgcn_permlane32_swap(__float_as_uint(v), __float_as_uint(v), false, false);
  return __uint_as_float(b[0]) + __uint_as_float(b[1]);
}
DI float gelu_tanh(float x) {
  const float c1 = -2.0f * 0.7978845608028654f * 1.4426950408889634f;
  const float c3 = c1 * 0.044715f;
  float t = x * __builtin_fmaf(c3, x * x, c1);
  float e = __builtin_amdgcn_exp2f(t);
  return x * __builtin_amdgcn_rcpf(1.0f + e);
}

DI void glds16(unsigned voff, const void* sbase, unsigned lds_dst) {
  unsigned keep;
  asm volatile("s_mov_b32 %0, m0\n\ts_mov_b32 m0, %3\n\ts_nop 0\n\tglobal_load_lds_dwordx4 %1, %2\n\ts_mov_b32 m0, %0"
               : "=&s"(keep) : "v"(voff), "s"(sbase), "s"(lds_dst) : "memory");
}
template <int N> DI void wait_vm() { asm volatile("s_waitcnt vmcnt(%0)" ::"i"(N) : "memory"); }
template <int N> DI void wait_vm_bar() {
  asm volatile("s_waitcnt vmcnt(%0) lgkmcnt(0)\n\ts_barrier" ::"i"(N) : "memory");
}
DI void bar_lds() { asm volatile("s_waitcnt lgkmcnt(0)\n\ts_barrier" ::: "memory"); }

template <int I, int N, class Fn> DI void static_for(Fn&& f) {
  if constexpr (I < N) {
    f(std::integral_constant<int, I>{});
    static_for<I + 1, N>(f);
  }
}

DI s16x4 tr_read(const LDSP char* p) {
  typedef short v4i16_t __attribute__((ext_vector_type(4)));
  return __builtin_bit_cast(s16x4, __builtin_amdgcn_ds_read_tr16_b64_v4i16((LDSP v4i16_t*)p));
}

constexpr int CH_A = 8192, CH_B = 8192;

template <int NTW, int ORI>
DI void chunk_mma(const LDSP char* a_base, const LDSP char* b_base, unsigned a_off, unsigned b_off, int wn,
                  f32x16* acc) {
#pragma unroll
  for (int s = 0; s < 4; ++s) {
    bf16x8 af = *(const LDSP bf16x8*)(a_base + (a_off ^ (s << 5)));
#pragma unroll
    for (int j = 0; j < NTW; ++j) {
      const int nt = wn * NTW + j;
      const LDSP char* bp = b_base + (nt >> 1) * CH_B + s * 2048;
      s16x4 lo = tr_read(bp + (b_off ^ ((nt & 1) << 6)));
      s16x4 hi = tr_read(bp + 512 + (b_off ^ ((nt & 1) << 6)));
      bf16x8 bf = __builtin_shufflevector(lo, hi, 0, 1, 2, 3, 4, 5, 6, 7);
      if ((ORI >> j) & 1) acc[j] = MFMA32(af, bf, acc[j]);
      else acc[j] = MFMA32(bf, af, acc[j]);
    }
  }
}
DI unsigned a_frag_off(int lane, int wm) {
  const int r = wm * 32 + (lane & 31), hh = lane >> 5;
  return r * 128 + ((hh ^ ((r >> 1) & 7)) << 4);
}
DI unsigned b_frag_off(int lane) {
  const int hh = lane >> 5, blk = (lane >> 4) & 1, q = (lane & 15) >> 2, p = lane & 3;
  const int r = 8 * hh + q;
  const int ch = 2 * blk + (p >> 1);
  return r * 128 + ((ch ^ (((r >> 1) & 1) << 2)) << 4) + 8 * (p & 1);
}

enum { EPI_WO = 0, EPI_W1 = 1, EPI_W2 = 2, EPI_W3 = 3, EPI_W4 = 4 };

struct EpiArgs {
  const u16* r0;
  const u16* r1;
  u16* out;
};

template <int EPI> DI void epi8(const EpiArgs& e, int row, int col, const float* v) {
  u32x4 o;
  if constexpr (EPI == EPI_WO) {
    u32x4 x = *(const u32x4*)(e.r0 + (size_t)row * D + col);
#pragma unroll
    for (int i = 0; i < 4; ++i)
      o[i] = pk_bf16(bf_lo(x[i]) + rbf(v[2 * i]), bf_hi(x[i]) + rbf(v[2 * i + 1]));
    *(u32x4*)(e.out + (size_t)row * D + col) = o;
  } else if constexpr (EPI == EPI_W1) {
#pragma unroll
    for (int i = 0; i < 4; ++i) o[i] = pk_bf16(gelu_tanh(rbf(v[2 * i])), gelu_tanh(rbf(v[2 * i + 1])));
    *(u32x4*)(e.out + (size_t)(row + 2) * D + col) = o;
  } else if constexpr (EPI == EPI_W2) {
    u32x4 x = *(const u32x4*)(e.r0 + (size_t)row * D + col);
#pragma unroll
    for (int i = 0; i < 4; ++i)
      o[i] = pk_bf16(bf_lo(x[i]) + rbf(v[2 * i]), bf_hi(x[i]) + rbf(v[2 * i + 1]));
    *(u32x4*)(e.out + (size_t)row * D + col) = o;
  } else if constexpr (EPI == EPI_W3) {
#pragma unroll
    for (int i = 0; i < 4; ++i) o[i] = pk_bf16(gelu_tanh(rbf(v[2 * i])), gelu_tanh(rbf(v[2 * i + 1])));
    *(u32x4*)(e.out + (size_t)row * FP + col) = o;
  } else {
    u32x4 h = *(const u32x4*)(e.r0 + (size_t)row * D + col);
    u32x4 x = *(const u32x4*)(e.r1 + (size_t)row * D + col);
#pragma unroll
    for (int i = 0; i < 4; ++i)
      o[i] = pk_bf16(bf_lo(h[i]) + rbf(bf_lo(x[i]) + rbf(v[2 * i])), bf_hi(h[i]) + rbf(bf_hi(x[i]) + rbf(v[2 * i + 1])));
    *(u32x4*)(e.out + (size_t)row * D + col) = o;
  }
}

template <int NTW, int EPI>
DI void epilogue_staged(char* smem, const f32x16* acc, int lane, int wm, int wn, int row0, int col0, int ncols,
                        const EpiArgs& e) {
  constexpr int BN = NTW * 64, LDW = BN + 4;
  float* st = (float*)smem;
  const int m = wm * 32 + (lane & 31), hh = lane >> 5;
#pragma unroll
  for (int j = 0; j < NTW; ++j)
#pragma unroll
    for (int g = 0; g < 4; ++g) {
      f32x4 v = {acc[j][4 * g], acc[j][4 * g + 1], acc[j][4 * g + 2], acc[j][4 * g + 3]};
      *(f32x4*)(st + m * LDW + (wn * NTW + j) * 32 + 8 * g + 4 * hh) = v;
    }
  __syncthreads();
  constexpr int CPR = BN / 8;
#pragma unroll
  for (int it = 0; it < (64 * CPR) / 256; ++it) {
    const int item = it * 256 + threadIdx.x, r = item / CPR, c = (item % CPR) * 8;
    if (c < ncols) {
      f32x4 v0 = *(const f32x4*)(st + r * LDW + c), v1 = *(const f32x4*)(st + r * LDW + c + 4);
      float v[8] = {v0[0], v0[1], v0[2], v0[3], v1[0], v1[1], v1[2], v1[3]};
      epi8<EPI>(e, row0 + r, col0 + c, v);
    }
  }
}

template <int NCH, int RING, int KW, int LDA, int EPI, bool SHIFT>
__global__ __launch_bounds__(256) void gemm_plain(const u16* __restrict__ A, const u16* __restrict__ W, EpiArgs e) {
  extern __shared__ __attribute__((aligned(16))) char smem[];
  const int tid = threadIdx.x, lane = tid & 63, wid = __builtin_amdgcn_readfirstlane(tid >> 6);
  const int b = blockIdx.x, rb = (b & 7) * 4 + ((b >> 3) & 3), cb = b >> 5;
  const int row0 = rb * 64, col0 = cb * 64;
  const unsigned lds0 = (unsigned)(uintptr_t)smem;
  constexpr int SLOT = CH_A + CH_B;
  unsigned aoff[2], boff[2];
  int krow[2];
#pragma unroll
  for (int i = 0; i < 2; ++i) {
    const int r = (2 * wid + i) * 8 + (lane >> 3), c = lane & 7;
    aoff[i] = (unsigned)(r * LDA + ((c ^ ((r >> 1) & 7)) << 3)) * 2u;
    krow[i] = r;
    boff[i] = (unsigned)(((c ^ (((r >> 1) & 1) << 2)) << 3)) * 2u;
  }
  const char* Ab = (const char*)(A + (size_t)row0 * LDA);
  const char* Wb = (const char*)(W + col0);
  auto issue = [&](auto CI) {
    constexpr int c = decltype(CI)::value;
    constexpr int slot = c % RING;
    constexpr int sh = !SHIFT ? 0 : ((c >> 1) == 0 ? 1 : (c >> 1) == 1 ? 3 : (c >> 1) == 2 ? 0 : 4);
    const char* ab = Ab + (size_t)sh * LDA * 2 + c * 128;
#pragma unroll
    for (int i = 0; i < 2; ++i) glds16(aoff[i], ab, lds0 + slot * SLOT + (2 * wid + i) * 1024);
#pragma unroll
    for (int i = 0; i < 2; ++i) {
      int kr = c * 64 + krow[i];
      if constexpr ((c + 1) * 64 > KW) kr = kr < KW ? kr : KW - 1;
      glds16(boff[i] + (unsigned)kr * (D * 2), Wb, lds0 + slot * SLOT + CH_A + (2 * wid + i) * 1024);
    }
  };
  static_for<0, (RING < NCH ? RING : NCH)>(issue);

  const int wm = wid >> 1, wn = wid & 1;
  const unsigned a_off = a_frag_off(lane, wm), b_off = b_frag_off(lane);
  f32x16 acc[1];
#pragma unroll
  for (int i = 0; i < 16; ++i) acc[0][i] = 0.f;
  static_for<0, NCH>([&](auto CI) {
    constexpr int c = decltype(CI)::value;
    constexpr int last = (c == 0) ? (RING - 1) : (c - 2 + RING);
    constexpr int lastc = last < NCH - 1 ? last : NCH - 1;
    wait_vm_bar<(lastc - c) * 4>();
    if constexpr (c >= 1 && c - 1 + RING < NCH) issue(std::integral_constant<int, c - 1 + RING>{});
    const LDSP char* base = (const LDSP char*)smem + (c % RING) * SLOT;
    chunk_mma<1, 0>(base, base + CH_A, a_off, b_off, wn, acc);
  });
  bar_lds();
  epilogue_staged<1, EPI>(smem, acc, lane, wm, wn, row0, col0, 64, e);
}

struct QkvOut { u16* q; u16* k; u16* v; };

template <int NTW, int ORI>
DI void qkv_epilogue(const f32x16* acc, int lane, int mt, int ntile0, const QkvOut& o) {
  const int i32 = lane & 31, hh = lane >> 5;
#pragma unroll
  for (int j = 0; j < NTW; ++j) {
    const int nt = ntile0 + j;
    const int part = nt >> 4;
    const int head0 = (nt & 15) * 2;
    if ((ORI >> j) & 1) {
      u16* dst = o.v + (size_t)(head0 + (i32 >> 4)) * 32768 + mt * 512 + hh * 128 + (i32 & 15) * 8;
#pragma unroll
      for (int s = 0; s < 2; ++s) {
        u32x4 w;
#pragma unroll
        for (int i = 0; i < 4; ++i) w[i] = pk_bf16(acc[j][8 * s + 2 * i], acc[j][8 * s + 2 * i + 1]);
        *(u32x4*)(dst + s * 256) = w;
      }
    } else {
      const float sc = part == 0 ? QSCALE : 1.0f;
      u16* dst = (part == 0 ? o.q : o.k) + (size_t)head0 * 32768 + mt * 512 + i32 * 8 + hh * 4;
#pragma unroll
      for (int g = 0; g < 4; ++g) {
        u32x2 w;
        w[0] = pk_bf16(acc[j][4 * g] * sc, acc[j][4 * g + 1] * sc);
        w[1] = pk_bf16(acc[j][4 * g + 2] * sc, acc[j][4 * g + 3] * sc);
        *(u32x2*)(dst + (g >> 1) * 32768 + (g & 1) * 256) = w;
      }
    }
  }
}

template <int MODE, int NT, int RING, int LDW>
__global__ __launch_bounds__(256) void gemm_ln(const u16* __restrict__ X, const u16* __restrict__ W, EpiArgs e,
                                               QkvOut qo, u16* __restrict__ xb_out) {
  extern __shared__ __attribute__((aligned(16))) char smem[];
  constexpr int NCH = 8, NTW = NT / 2, NBW = NT;
  constexpr int BSLOT = (NT / 2) * CH_B, LDS_B = NCH * CH_A;
  const int tid = threadIdx.x, lane = tid & 63, wid = __builtin_amdgcn_readfirstlane(tid >> 6);
  const int b = blockIdx.x, rb = (b & 7) * 4 + ((b >> 3) & 3), cb = b >> 5;
  const int row0 = rb * 64, col0 = cb * (NT * 32);
  const unsigned lds0 = (unsigned)(uintptr_t)smem;

  u32x4 xr[16];
  {
    const char* xp = (const char*)(X + (size_t)(row0 + wid * 16) * D);
    const unsigned xo = lane * 16;
#pragma unroll
    for (int r = 0; r < 16; ++r)
      asm volatile("global_load_dwordx4 %0, %1, %2 offset:%3"
                   : "=v"(xr[r]) : "v"(xo), "s"(xp + (r >> 2) * 4096), "i"((r & 3) * 1024) : "memory");
  }
  unsigned boff[NBW];
#pragma unroll
  for (int i = 0; i < NBW; ++i) {
    const int p = wid * NBW + i, st = p >> 3, r = (p & 7) * 8 + (lane >> 3), c = lane & 7;
    int col = col0 + st * 64 + ((c ^ (((r >> 1) & 1) << 2)) << 3);
    if (MODE == 2) col = col < LDW - 8 ? col : LDW - 8;
    boff[i] = (unsigned)(r * LDW + col) * 2u;
  }
  auto issue = [&](auto CI) {
    constexpr int c = decltype(CI)::value;
    const char* wb = (const char*)W + (size_t)c * 64 * LDW * 2;
#pragma unroll
    for (int i = 0; i < NBW; ++i) glds16(boff[i], wb, lds0 + LDS_B + (c % RING) * BSLOT + (wid * NBW + i) * 1024);
  };
  static_for<0, RING>(issue);

  asm volatile("s_waitcnt vmcnt(%16)"
               : "+v"(xr[0]), "+v"(xr[1]), "+v"(xr[2]), "+v"(xr[3]), "+v"(xr[4]), "+v"(xr[5]), "+v"(xr[6]), "+v"(xr[7]),
                 "+v"(xr[8]), "+v"(xr[9]), "+v"(xr[10]), "+v"(xr[11]), "+v"(xr[12]), "+v"(xr[13]), "+v"(xr[14]),
                 "+v"(xr[15])
               : "i"(RING * NBW)
               : "memory");
#pragma unroll
  for (int r = 0; r < 16; ++r) {
    const int row = wid * 16 + r;
    float v[8];
#pragma unroll
    for (int i = 0; i < 4; ++i) { v[2 * i] = bf_lo(xr[r][i]); v[2 * i + 1] = bf_hi(xr[r][i]); }
#pragma unroll
    for (int pass = 0; pass < (MODE == 1 ? 2 : 1); ++pass) {
      float s1 = 0.f;
#pragma unroll
      for (int i = 0; i < 8; ++i) s1 += v[i];
      const float mean = wave_sum(s1) * (1.0f / D);
      float s2 = 0.f;
#pragma unroll
      for (int i = 0; i < 8; ++i) { v[i] -= mean; s2 = __builtin_fmaf(v[i], v[i], s2); }
      const float var = wave_sum(s2) * (1.0f / D);
      const float rs = __builtin_amdgcn_rsqf(var + LN_EPS);
      u32x4 o;
#pragma unroll
      for (int i = 0; i < 4; ++i) o[i] = pk_bf16(v[2 * i] * rs, v[2 * i + 1] * rs);
      if (MODE == 1 && pass == 0) {
#pragma unroll
        for (int i = 0; i < 4; ++i) { v[2 * i] = bf_lo(o[i]); v[2 * i + 1] = bf_hi(o[i]); }
        if ((lane >> 3) == cb) *(u32x4*)(xb_out + (size_t)(row0 + row) * D + lane * 8) = o;
      } else {
        *(LDSP u32x4*)((LDSP char*)smem + (lane >> 3) * CH_A + row * 128 + (((lane & 7) ^ ((row >> 1) & 7)) << 4)) = o;
      }
    }
  }
  if (MODE == 1) {
    if ((rb == 0 || rb == 31) && tid < 16) {
      const int hr = (rb == 0 ? 0 : S + 2) + (tid >> 3);
      *(u32x4*)(e.out + (size_t)hr * D + col0 + (tid & 7) * 8) = u32x4{0u, 0u, 0u, 0u};
    }
  }

  const int wm = wid >> 1, wn = wid & 1;
  const unsigned a_off = a_frag_off(lane, wm), b_off = b_frag_off(lane);
  f32x16 acc[NTW];
#pragma unroll
  for (int j = 0; j < NTW; ++j)
#pragma unroll
    for (int i = 0; i < 16; ++i) acc[j][i] = 0.f;

  auto mainloop = [&](auto ORIc) {
    constexpr int ORI = decltype(ORIc)::value;
    static_for<0, NCH>([&](auto CI) {
      constexpr int c = decltype(CI)::value;
      constexpr int last = (c == 0) ? (RING - 1) : (c - 2 + RING);
      constexpr int lastc = last < NCH - 1 ? last : NCH - 1;
      wait_vm_bar<(lastc - c) * NBW>();
      if constexpr (c >= 1 && c - 1 + RING < NCH) issue(std::integral_constant<int, c - 1 + RING>{});
      chunk_mma<NTW, ORI>((const LDSP char*)smem + c * CH_A, (const LDSP char*)smem + LDS_B + (c % RING) * BSLOT, a_off,
                          b_off, wn, acc);
    });
  };
  if constexpr (MODE == 0) {
    const int ntile0 = cb * NT + wn * NTW;
    const int mt = rb * 2 + wm;
    if (ntile0 + NTW <= 32) { mainloop(std::integral_constant<int, 0>{}); qkv_epilogue<NTW, 0>(acc, lane, mt, ntile0, qo); }
    else if (ntile0 >= 32) { mainloop(std::integral_constant<int, 7>{}); qkv_epilogue<NTW, 7>(acc, lane, mt, ntile0, qo); }
    else { mainloop(std::integral_constant<int, 4>{}); qkv_epilogue<NTW, 4>(acc, lane, mt, ntile0, qo); }
  } else {
    mainloop(std::integral_constant<int, 0>{});
    bar_lds();
    if constexpr (MODE == 1) epilogue_staged<NTW, EPI_W1>(smem, acc, lane, wm, wn, row0, col0, 64, e);
    else {
      if (col0 + NT * 32 > F) {
#pragma unroll
        for (int j = 0; j < NTW; ++j)
          if (col0 + (wn * NTW + j) * 32 >= F)
#pragma unroll
            for (int i = 0; i < 16; ++i) acc[j][i] = 0.f;
      }
      epilogue_staged<NTW, EPI_W3>(smem, acc, lane, wm, wn, row0, col0, FP - col0, e);
    }
  }
}

constexpr int AT_K = 0, AT_V = 65536, AT_C = 131072, AT_LDS = AT_C + 2048;

__global__ __launch_bounds__(512) void attn_kernel(const u16* __restrict__ Qf, const u16* __restrict__ Kf,
                                                   const u16* __restrict__ Vf, u16* __restrict__ O) {
  extern __shared__ __attribute__((aligned(16))) char smem[];
  const int tid = threadIdx.x, lane = tid & 63, wid = __builtin_amdgcn_readfirstlane(tid >> 6);
  const int b = blockIdx.x, head = (b & 7) * 4 + ((b >> 3) & 3), qb = b >> 5;
  const unsigned lds0 = (unsigned)(uintptr_t)smem;
  const int r32 = lane & 31, hh = lane >> 5;
  const int qt = qb * 8 + wid;

  bf16x8 qf;
  {
    const u16* qp = Qf + (size_t)head * 32768 + qt * 512 + lane * 8;
    asm volatile("global_load_dwordx4 %0, %1, off" : "=v"(qf) : "v"(qp) : "memory");
  }
  const char* kb = (const char*)(Kf + (size_t)head * 32768) + wid * 1024;
  const char* vb = (const char*)(Vf + (size_t)head * 32768) + wid * 1024;
#pragma unroll
  for (int g = 0; g < 8; ++g) glds16(lane * 16, kb + g * 8192, lds0 + AT_K + g * 8192 + wid * 1024);
#pragma unroll
  for (int g = 0; g < 8; ++g) glds16(lane * 16, vb + g * 8192, lds0 + AT_V + g * 8192 + wid * 1024);
  if (tid < 64) {
    const unsigned w = ((tid & 15) == 0) ? 0x3f803f80u : 0u;
    *(LDSP u32x4*)((LDSP char*)smem + AT_C + (tid >> 4) * 512 + (tid & 15) * 16) = u32x4{w, w, w, w};
  }
  asm volatile("s_waitcnt vmcnt(16)" : "+v"(qf)::"memory");

  float mx = -3.0e38f;
  const LDSP char* kp = (const LDSP char*)smem + AT_K + lane * 16;
  static_for<0, 8>([&](auto GI) {
    constexpr int g = decltype(GI)::value;
    wait_vm_bar<15 - g>();
#pragma unroll
    for (int i = 0; i < 8; ++i) {
      bf16x8 kf = *(const LDSP bf16x8*)(kp + (g * 8 + i) * 1024);
      f32x16 z;
#pragma unroll
      for (int r = 0; r < 16; ++r) z[r] = 0.f;
      f32x16 s = MFMA32(kf, qf, z);
#pragma unroll
      for (int r = 0; r < 16; r += 2) mx = __builtin_fmaxf(__builtin_fmaxf(mx, s[r]), s[r + 1]);
    }
  });
  {
    auto rr = __builtin_amdgcn_permlane32_swap(__float_as_uint(mx), __float_as_uint(mx), false, false);
    mx = __builtin_fmaxf(__uint_as_float(rr[0]), __uint_as_float(rr[1]));
  }
  f32x16 nm;
#pragma unroll
  for (int r = 0; r < 16; ++r) nm[r] = -mx;

  unsigned vaddr, vinc;
  if (r32 < 16) { vaddr = AT_V + hh * 256 + r32 * 16; vinc = 2048; }
  else { vaddr = AT_C + (r32 == 16 ? 0 : (r32 >= 20 && r32 < 28) ? 64 : 16); vinc = 0; }
  f32x16 o;
#pragma unroll
  for (int r = 0; r < 16; ++r) o[r] = 0.f;
  wait_vm_bar<0>();
  const LDSP char* lb = (const LDSP char*)smem;
#pragma unroll 1
  for (int t = 0; t < 32; ++t) {
    bf16x8 k0 = *(const LDSP bf16x8*)(kp + t * 2048);
    bf16x8 k1 = *(const LDSP bf16x8*)(kp + t * 2048 + 1024);
    bf16x8 v0 = *(const LDSP bf16x8*)(lb + vaddr);
    bf16x8 v1 = *(const LDSP bf16x8*)(lb + vaddr + 512);
    bf16x8 v2 = *(const LDSP bf16x8*)(lb + vaddr + 1024);
    bf16x8 v3 = *(const LDSP bf16x8*)(lb + vaddr + 1536);
    vaddr += vinc;
    f32x16 s0 = MFMA32(k0, qf, nm);
    f32x16 s1 = MFMA32(k1, qf, nm);
    u32x4 p0, p1, p2, p3;
#pragma unroll
    for (int i = 0; i < 4; ++i) {
      p0[i] = pk_bf16(__builtin_amdgcn_exp2f(s0[2 * i]), __builtin_amdgcn_exp2f(s0[2 * i + 1]));
      p1[i] = pk_bf16(__builtin_amdgcn_exp2f(s0[8 + 2 * i]), __builtin_amdgcn_exp2f(s0[8 + 2 * i + 1]));
    }
    o = MFMA32(v0, __builtin_bit_cast(bf16x8, p0), o);
    o = MFMA32(v1, __builtin_bit_cast(bf16x8, p1), o);
#pragma unroll
    for (int i = 0; i < 4; ++i) {
      p2[i] = pk_bf16(__builtin_amdgcn_exp2f(s1[2 * i]), __builtin_amdgcn_exp2f(s1[2 * i + 1]));
      p3[i] = pk_bf16(__builtin_amdgcn_exp2f(s1[8 + 2 * i]), __builtin_amdgcn_exp2f(s1[8 + 2 * i + 1]));
    }
    o = MFMA32(v2, __builtin_bit_cast(bf16x8, p2), o);
    o = MFMA32(v3, __builtin_bit_cast(bf16x8, p3), o);
  }
  float l;
  {
    auto rr = __builtin_amdgcn_permlane32_swap(__float_as_uint(o[8]), __float_as_uint(o[8]), false, false);
    l = __uint_as_float(rr[0]);
    l = hh ? l : o[8];
  }
  const float inv = 1.0f / l;
  u16* op = O + (size_t)(qt * 32 + r32) * D + head * 16 + hh * 4;
  u32x2 w0, w1;
  w0[0] = pk_bf16(o[0] * inv, o[1] * inv); w0[1] = pk_bf16(o[2] * inv, o[3] * inv);
  w1[0] = pk_bf16(o[4] * inv, o[5] * inv); w1[1] = pk_bf16(o[6] * inv, o[7] * inv);
  *(u32x2*)op = w0;
  *(u32x2*)(op + 8) = w1;
}

extern "C" void kernel_launch(void* const* d_in, const int* in_sizes, int n_in, void* d_out, int out_size, void* d_ws,
                              size_t ws_size, hipStream_t stream) {
  const u16* x = (const u16*)d_in[0];
  const u16* wqkv = (const u16*)d_in[1];
  const u16* wo = (const u16*)d_in[2];
  const u16* w1 = (const u16*)d_in[4];
  const u16* w2 = (const u16*)d_in[5];
  const u16* w3 = (const u16*)d_in[6];
  const u16* w4 = (const u16*)d_in[7];
  u16* out = (u16*)d_out;
  char* ws = (char*)d_ws;
  const size_t MB = 1u << 20;
  u16* q = (u16*)(ws + 0 * MB);
  u16* k = (u16*)(ws + 2 * MB);
  u16* v = (u16*)(ws + 4 * MB);
  u16* o = (u16*)(ws + 6 * MB);
  u16* h = (u16*)(ws + 8 * MB);
  u16* xb = (u16*)(ws + 10 * MB);
  u16* g = (u16*)(ws + 12 * MB);
  u16* x1 = (u16*)(ws + 15 * MB);
  u16* u = (u16*)(ws + 17 * MB);

  constexpr int R6 = 3;
  constexpr int LDS_LN6 = 8 * CH_A + R6 * 3 * CH_B;
  constexpr int LDS_LN2 = 8 * CH_A + 8 * CH_B;
  constexpr int RP = 8;
  constexpr int LDS_PL = RP * (CH_A + CH_B);

  auto k_qkv = gemm_ln<0, 6, R6, 3 * D>;
  auto k_w1 = gemm_ln<1, 2, 8, D>;
  auto k_w3 = gemm_ln<2, 6, R6, F>;
  auto k_wo = gemm_plain<8, RP, D, D, EPI_WO, false>;
  auto k_w2 = gemm_plain<8, RP, D, D, EPI_W2, true>;
  auto k_w4 = gemm_plain<FP / 64, RP, F, FP, EPI_W4, false>;
  (void)hipFuncSetAttribute((const void*)k_qkv, hipFuncAttributeMaxDynamicSharedMemorySize, LDS_LN6);
  (void)hipFuncSetAttribute((const void*)k_w1, hipFuncAttributeMaxDynamicSharedMemorySize, LDS_LN2);
  (void)hipFuncSetAttribute((const void*)k_w3, hipFuncAttributeMaxDynamicSharedMemorySize, LDS_LN6);
  (void)hipFuncSetAttribute((const void*)k_wo, hipFuncAttributeMaxDynamicSharedMemorySize, LDS_PL);
  (void)hipFuncSetAttribute((const void*)k_w2, hipFuncAttributeMaxDynamicSharedMemorySize, LDS_PL);
  (void)hipFuncSetAttribute((const void*)k_w4, hipFuncAttributeMaxDynamicSharedMemorySize, LDS_PL);
  (void)hipFuncSetAttribute((const void*)attn_kernel, hipFuncAttributeMaxDynamicSharedMemorySize, AT_LDS);

  QkvOut qo{q, k, v};
  EpiArgs e0{nullptr, nullptr, nullptr};
  k_qkv<<<256, 256, LDS_LN6, stream>>>(x, wqkv, e0, qo, nullptr);
  attn_kernel<<<256, 512, AT_LDS, stream>>>(q, k, v, o);
  k_wo<<<256, 256, LDS_PL, stream>>>(o, wo, EpiArgs{x, nullptr, h});
  k_w1<<<256, 256, LDS_LN2, stream>>>(h, w1, EpiArgs{nullptr, nullptr, g}, qo, xb);
  k_w2<<<256, 256, LDS_PL, stream>>>(g, w2, EpiArgs{xb, nullptr, x1});
  k_w3<<<256, 256, LDS_LN6, stream>>>(x1, w3, EpiArgs{nullptr, nullptr, u}, qo, nullptr);
  k_w4<<<256, 256, LDS_PL, stream>>>(u, w4, EpiArgs{h, x1, out});
}
```

```cpp
#include <hip/hip_runtime.h>
#include <hip/hip_bf16.h>
#include <stdint.h>

typedef __attribute__((ext_vector_type(8))) short bf16x8;
typedef __attribute__((ext_vector_type(4))) short s16x4;
typedef __attribute__((ext_vector_type(16))) float f32x16;
typedef __attribute__((ext_vector_type(4))) float f32x4;
typedef __attribute__((ext_vector_type(2))) float f32x2;
typedef __attribute__((ext_vector_type(2))) __bf16 bf16x2_t;
typedef __attribute__((ext_vector_type(4))) unsigned u32x4;
typedef __attribute__((ext_vector_type(2))) unsigned u32x2;
typedef unsigned short u16;

#define DI __device__ __forceinline__
#define LDSP __attribute__((address_space(3)))
#define MFMA32(a, b, c) __builtin_amdgcn_mfma_f32_32x32x16_bf16((a), (b), (c), 0, 0, 0)
#define SCHED0() __builtin_amdgcn_sched_barrier(0)

constexpr int S = 2048, D = 512, NH = 32, F = 1376, FP = 1408;
constexpr float LN_EPS = 1e-5f;
constexpr float QSCALE = 0.25f * 1.4426950408889634f;

DI unsigned pk_bf16(float a, float b) {
  f32x2 v = {a, b};
  bf16x2_t r = __builtin_convertvector(v, bf16x2_t);
  return __builtin_bit_cast(unsigned, r);
}
DI float bf_lo(unsigned u) { return __uint_as_float(u << 16); }
DI float bf_hi(unsigned u) { return __uint_as_float(u & 0xffff0000u); }
DI float rbf(float x) { return bf_lo(pk_bf16(x, x)); }
DI float dot2(unsigned a, unsigned b, float c) {
  return __builtin_amdgcn_fdot2_f32_bf16(__builtin_bit_cast(bf16x2_t, a), __builtin_bit_cast(bf16x2_t, b), c, false);
}
template <int CTRL> DI float dpp_add(float v) {
  int r = __builtin_amdgcn_update_dpp(0, __float_as_int(v), CTRL, 0xf, 0xf, true);
  return v + __int_as_float(r);
}
DI float gelu_tanh(float x) {
  const float c1 = -2.0f * 0.7978845608028654f * 1.4426950408889634f;
  const float c3 = c1 * 0.044715f;
  float t = x * __builtin_fmaf(c3, x * x, c1);
  float e = __builtin_amdgcn_exp2f(t);
  return x * __builtin_amdgcn_rcpf(1.0f + e);
}

DI void glds16(unsigned voff, const void* sbase, unsigned lds_dst) {
  unsigned keep;
  asm volatile("s_mov_b32 %0, m0\n\ts_mov_b32 m0, %3\n\ts_nop 0\n\tglobal_load_lds_dwordx4 %1, %2\n\ts_mov_b32 m0, %0"
               : "=&s"(keep) : "v"(voff), "s"(sbase), "s"(lds_dst) : "memory");
}
template <int N> DI void wait_vm_bar() {
  asm volatile("s_waitcnt vmcnt(%0) lgkmcnt(0)\n\ts_barrier" ::"i"(N) : "memory");
}
DI void bar_lds() { asm volatile("s_waitcnt lgkmcnt(0)\n\ts_barrier" ::: "memory"); }
template <int I, int N, class Fn> DI void static_for(Fn&& f) {
  if constexpr (I < N) {
    f(std::integral_constant<int, I>{});
    static_for<I + 1, N>(f);
  }
}
DI s16x4 tr_read(const LDSP char* p) {
  typedef short v4i16_t __attribute__((ext_vector_type(4)));
  return __builtin_bit_cast(s16x4, __builtin_amdgcn_ds_read_tr16_b64_v4i16((LDSP v4i16_t*)p));
}

constexpr int CH = 8192;
enum { EPI_WO = 1, EPI_W1 = 2, EPI_W2 = 3, EPI_W4 = 5 };

struct GArgs {
  const u16* A;
  const u16* W;
  const u16* r0;
  const u16* r1;
  u16* o0;
  u16* o1;
  u16* o2;
  float* stats;
  float* part;
  const float* csum;
};

DI unsigned b_lane_off(int lane) {
  const int hh = lane >> 5, blk = (lane >> 4) & 1, q = (lane & 15) >> 2, p = lane & 3;
  const int kr = 8 * hh + q;
  return kr * 128 + (((2 * blk + (p >> 1)) ^ (((kr >> 1) & 1) << 2)) << 4) + 8 * (p & 1);
}
DI u32x4 b_frag(const LDSP char* bp) {
  const u32x2 lo = __builtin_bit_cast(u32x2, tr_read(bp));
  const u32x2 hi = __builtin_bit_cast(u32x2, tr_read(bp + 512));
  return u32x4{lo[0], lo[1], hi[0], hi[1]};
}
DI f32x2 row8_stats(const u32x4& o) {
  float a = 0.f, q = 0.f;
#pragma unroll
  for (int i = 0; i < 4; ++i) { a = dot2(o[i], 0x3f803f80u, a); q = dot2(o[i], o[i], q); }
  a = dpp_add<0xB1>(a); q = dpp_add<0xB1>(q);
  a = dpp_add<0x4E>(a); q = dpp_add<0x4E>(q);
  a = dpp_add<0x141>(a); q = dpp_add<0x141>(q);
  return f32x2{a, q};
}

template <int NCH, int RING, int PH, int LDA, int KW, int EPI>
__global__ __launch_bounds__(512) void gemm_n(GArgs g) {
  extern __shared__ __attribute__((aligned(16))) char smem[];
  constexpr bool LN = (EPI == EPI_W1);
  constexpr bool SHIFT = (EPI == EPI_W2);
  constexpr int SLOT = 2 * CH, LDW = D;
  const int tid = threadIdx.x, lane = tid & 63, wid = __builtin_amdgcn_readfirstlane(tid >> 6);
  const int b = blockIdx.x, rb = (b & 7) * 4 + ((b >> 3) & 3), cb = b >> 5;
  const int row0 = rb * 64, col0 = cb * 64;
  const unsigned lds0 = (unsigned)(uintptr_t)smem;

  const int er = tid >> 3, ec = (tid & 7) * 8;
  const size_t eo = (size_t)(row0 + er) * D + col0 + ec;
  u32x4 rv0, rv1;
  f32x2 rst;
  f32x4 cv0, cv1;
  if constexpr (EPI == EPI_WO || EPI == EPI_W2 || EPI == EPI_W4) rv0 = *(const u32x4*)(g.r0 + eo);
  if constexpr (EPI == EPI_W4) rv1 = *(const u32x4*)(g.r1 + eo);
  if constexpr (EPI == EPI_W2) rst = *(const f32x2*)(g.stats + (row0 + er) * 2);
  if constexpr (LN) {
    rst = *(const f32x2*)(g.part + ((row0 + er) * 8 + (tid & 7)) * 2);
    const float* cp = g.csum + col0 + ec;
    cv0 = (*(const f32x4*)(cp) + *(const f32x4*)(cp + (D + FP))) +
          (*(const f32x4*)(cp + 2 * (D + FP)) + *(const f32x4*)(cp + 3 * (D + FP)));
    cv1 = (*(const f32x4*)(cp + 4) + *(const f32x4*)(cp + 4 + (D + FP))) +
          (*(const f32x4*)(cp + 4 + 2 * (D + FP)) + *(const f32x4*)(cp + 4 + 3 * (D + FP)));
  }

  const int sr = wid * 8 + (lane >> 3), sc = lane & 7;
  const unsigned aoff = (unsigned)(sr * LDA + ((sc ^ ((sr >> 1) & 7)) << 3)) * 2u;
  const unsigned boff = (unsigned)(col0 + ((sc ^ (((sr >> 1) & 1) << 2)) << 3)) * 2u;
  const char* Ab = (const char*)(g.A + (size_t)row0 * LDA);
  auto issue = [&](auto CI) {
    constexpr int c = decltype(CI)::value, slot = c % RING;
    constexpr int sh = SHIFT ? ((0x4031 >> (((c >> 1) & 3) * 4)) & 15) : 0;
    const unsigned dst = lds0 + slot * SLOT + wid * 1024;
    glds16(aoff, Ab + (size_t)sh * (LDA * 2) + c * 128, dst);
    int kr = c * 64 + sr;
    if ((c + 1) * 64 > KW) kr = kr < KW ? kr : KW - 1;
    glds16(boff + (unsigned)kr * (LDW * 2), g.W, dst + CH);
  };
  constexpr int PRE = RING < NCH ? RING : NCH;
  static_for<0, PRE>(issue);
  const int kg = wid >> 2, wm = (wid >> 1) & 1, wn = wid & 1;
  const int i32 = lane & 31, hh = lane >> 5;
  const int ar = wm * 32 + i32;
  const unsigned a_off = ar * 128 + (((4 * kg + hh) ^ ((ar >> 1) & 7)) << 4);
  const unsigned b_off = CH + kg * 4096 + (b_lane_off(lane) ^ (wn << 6));
  f32x16 acc;
#pragma unroll
  for (int r = 0; r < 16; ++r) acc[r] = 0.f;
  static_assert(RING % PH == 0 && RING >= 2 * PH && NCH % PH == 0, "ring / phase geometry");
  static_for<0, NCH>([&](auto CI) {
    constexpr int c = decltype(CI)::value, slot = c % RING;
    if constexpr (c % PH == 0) {
      constexpr int issued0 = RING + (c >= PH ? c - PH : 0);
      constexpr int issued = issued0 < NCH ? issued0 : NCH;
      wait_vm_bar<(issued - c - PH) * 2>();
      if constexpr (c >= PH)
        static_for<0, PH>([&](auto PI) {
          constexpr int cn = c - PH + RING + decltype(PI)::value;
          if constexpr (cn < NCH) issue(std::integral_constant<int, cn>{});
        });
    }
    const LDSP char* base = (const LDSP char*)smem + slot * SLOT;
#pragma unroll
    for (int s = 0; s < 2; ++s) {
      const u32x4 af = *(const LDSP u32x4*)(base + (a_off ^ (s << 5)));
      const u32x4 bf = b_frag(base + s * 2048 + b_off);
      acc = MFMA32(__builtin_bit_cast(bf16x8, bf), __builtin_bit_cast(bf16x8, af), acc);
    }
  });
  bar_lds();

  constexpr int LDS_ = 68;
  float* st = (float*)smem;
#pragma unroll
  for (int q = 0; q < 4; ++q) {
    f32x4 v = {acc[4 * q], acc[4 * q + 1], acc[4 * q + 2], acc[4 * q + 3]};
    *(f32x4*)(st + (kg * 64 + ar) * LDS_ + wn * 32 + 8 * q + 4 * hh) = v;
  }
  __syncthreads();
  float v[8];
  {
    const f32x4 a0 = *(const f32x4*)(st + er * LDS_ + ec), a1 = *(const f32x4*)(st + er * LDS_ + ec + 4);
    const f32x4 b0 = *(const f32x4*)(st + (64 + er) * LDS_ + ec), b1 = *(const f32x4*)(st + (64 + er) * LDS_ + ec + 4);
#pragma unroll
    for (int i = 0; i < 4; ++i) { v[i] = a0[i] + b0[i]; v[4 + i] = a1[i] + b1[i]; }
  }
  u32x4 o;
  if constexpr (EPI == EPI_WO) {
#pragma unroll
    for (int i = 0; i < 4; ++i) o[i] = pk_bf16(bf_lo(rv0[i]) + rbf(v[2 * i]), bf_hi(rv0[i]) + rbf(v[2 * i + 1]));
    *(u32x4*)(g.o0 + eo) = o;
    const f32x2 ps = row8_stats(o);
    if ((tid & 7) == 0) *(f32x2*)(g.part + ((row0 + er) * 8 + cb) * 2) = ps;
  } else if constexpr (EPI == EPI_W1) {
    float a = rst[0], q = rst[1];
    a = dpp_add<0xB1>(a); q = dpp_add<0xB1>(q);
    a = dpp_add<0x4E>(a); q = dpp_add<0x4E>(q);
    a = dpp_add<0x141>(a); q = dpp_add<0x141>(q);
    const float mean = a * (1.0f / D);
    const float var = fmaxf(q * (1.0f / D) - mean * mean, 0.f);
    float rs = __builtin_amdgcn_rsqf(var + LN_EPS);
    if (cb == 0 && (tid & 7) == 0) *(f32x2*)(g.stats + (row0 + er) * 2) = f32x2{mean, rs};
    rs *= __builtin_amdgcn_rsqf(var * rs * rs + LN_EPS);
    const float rm = rs * mean;
#pragma unroll
    for (int i = 0; i < 4; ++i) {
      v[i] = __builtin_fmaf(v[i], rs, -rm * cv0[i]);
      v[4 + i] = __builtin_fmaf(v[4 + i], rs, -rm * cv1[i]);
    }
#pragma unroll
    for (int i = 0; i < 4; ++i) o[i] = pk_bf16(gelu_tanh(rbf(v[2 * i])), gelu_tanh(rbf(v[2 * i + 1])));
    *(u32x4*)(g.o0 + eo + 2 * D) = o;
    if ((rb == 0 || rb == 31) && tid < 16) {
      const int hr = (rb == 0 ? 0 : S + 2) + (tid >> 3);
      *(u32x4*)(g.o0 + (size_t)hr * D + col0 + ec) = u32x4{0u, 0u, 0u, 0u};
    }
  } else if constexpr (EPI == EPI_W2) {
#pragma unroll
    for (int i = 0; i < 4; ++i) {
      const float xl = rbf((bf_lo(rv0[i]) - rst[0]) * rst[1]), xh = rbf((bf_hi(rv0[i]) - rst[0]) * rst[1]);
      o[i] = pk_bf16(xl + rbf(v[2 * i]), xh + rbf(v[2 * i + 1]));
    }
    *(u32x4*)(g.o0 + eo) = o;
    const f32x2 ps = row8_stats(o);
    if ((tid & 7) == 0) *(f32x2*)(g.part + ((row0 + er) * 8 + cb) * 2) = ps;
  } else {
#pragma unroll
    for (int i = 0; i < 4; ++i)
      o[i] = pk_bf16(bf_lo(rv0[i]) + rbf(bf_lo(rv1[i]) + rbf(v[2 * i])),
                     bf_hi(rv0[i]) + rbf(bf_hi(rv1[i]) + rbf(v[2 * i + 1])));
    *(u32x4*)(g.o0 + eo) = o;
  }
}

constexpr int W_RING = 4, W_SLOT = 4 * CH, W_TAB = W_RING * W_SLOT;
constexpr int W_LDS = W_TAB + 16384;

template <bool QKV, int LDW>
__global__ __launch_bounds__(512) void gemm_w(GArgs g) {
  extern __shared__ __attribute__((aligned(16))) char smem[];
  constexpr int NCH = 8, RING = W_RING, PH = 2, SLOT = W_SLOT;
  const int tid = threadIdx.x, lane = tid & 63, wid = __builtin_amdgcn_readfirstlane(tid >> 6);
  const int b = blockIdx.x, rb = (b & 7) * 2 + ((b >> 3) & 1), cb = b >> 4;
  const int row0 = rb * 128, col0 = cb * 128;
  const unsigned lds0 = (unsigned)(uintptr_t)smem;
  float* rowt = (float*)(smem + W_TAB);
  float* colt = rowt + 256;
  float* rowp = colt + 128;
  float* colp = rowp + 2048;

  f32x4 pv[4];
  float cvv[4];
  if constexpr (!QKV) {
    const float* pp = g.part + (row0 + (tid & 127)) * 16;
    const float* cp = g.csum + col0 + (tid & 127);
#pragma unroll
    for (int i = 0; i < 4; ++i)
      asm volatile("global_load_dwordx4 %0, %1, off offset:%2" : "=v"(pv[i]) : "v"(pp), "i"(16 * i) : "memory");
#pragma unroll
    for (int i = 0; i < 4; ++i)
      asm volatile("global_load_dword %0, %1, off" : "=v"(cvv[i]) : "v"(cp + i * (D + FP)) : "memory");
  }
  const int sr = wid * 8 + (lane >> 3), sc = lane & 7;
  const unsigned aoff = (unsigned)(sr * D + ((sc ^ ((sr >> 1) & 7)) << 3)) * 2u;
  unsigned boff[2];
#pragma unroll
  for (int st = 0; st < 2; ++st) {
    int col = col0 + st * 64 + ((sc ^ (((sr >> 1) & 1) << 2)) << 3);
    if (!QKV) col = col < LDW - 8 ? col : LDW - 8;
    boff[st] = (unsigned)(sr * LDW + col) * 2u;
  }
  const char* Ab = (const char*)(g.A + (size_t)row0 * D);
  auto issue = [&](auto CI) {
    constexpr int c = decltype(CI)::value, slot = c % RING;
    const unsigned dst = lds0 + slot * SLOT + wid * 1024;
    glds16(aoff, Ab + c * 128, dst);
    glds16(aoff, Ab + 64 * D * 2 + c * 128, dst + CH);
    const char* wb = (const char*)g.W + (size_t)c * 64 * LDW * 2;
    glds16(boff[0], wb, dst + 2 * CH);
    glds16(boff[1], wb, dst + 3 * CH);
  };
  static_for<0, RING>(issue);

  if constexpr (!QKV) {
    asm volatile("s_waitcnt vmcnt(16)" : "+v"(pv[0]), "+v"(pv[1]), "+v"(pv[2]), "+v"(pv[3]),
                 "+v"(cvv[0]), "+v"(cvv[1]), "+v"(cvv[2]), "+v"(cvv[3])::"memory");
    if (tid < 128) {
      const float a = (pv[0][0] + pv[0][2]) + (pv[1][0] + pv[1][2]) + (pv[2][0] + pv[2][2]) + (pv[3][0] + pv[3][2]);
      const float q = (pv[0][1] + pv[0][3]) + (pv[1][1] + pv[1][3]) + (pv[2][1] + pv[2][3]) + (pv[3][1] + pv[3][3]);
      const float mean = a * (1.0f / D);
      const float rs = __builtin_amdgcn_rsqf(fmaxf(q * (1.0f / D) - mean * mean, 0.f) + LN_EPS);
      *(f32x2*)(rowt + tid * 2) = f32x2{rs, rs * mean};
    } else if (tid < 256) {
      colt[tid - 128] = (cvv[0] + cvv[1]) + (cvv[2] + cvv[3]);
    }
  }

  const int wm = wid >> 2, wn = wid & 3;
  const int i32 = lane & 31, hh = lane >> 5;
  const int ar = wm * 64 + i32;
  const unsigned a_off = ar * 128 + ((hh ^ ((ar >> 1) & 7)) << 4);
  const unsigned b_off = 2 * CH + (wn >> 1) * CH + (b_lane_off(lane) ^ ((wn & 1) << 6));
  f32x16 acc[2];
  float s1[2][2], s2[2][2], cs[2];
#pragma unroll
  for (int i = 0; i < 2; ++i) {
    s1[i][0] = s1[i][1] = s2[i][0] = s2[i][1] = cs[i] = 0.f;
#pragma unroll
    for (int r = 0; r < 16; ++r) acc[i][r] = 0.f;
  }
  auto mainloop = [&](auto VO) {
    constexpr bool VOR = decltype(VO)::value;
    static_for<0, NCH>([&](auto CI) {
      constexpr int c = decltype(CI)::value, slot = c % RING;
      if constexpr (c % PH == 0) {
        constexpr int issued0 = RING + (c >= PH ? c - PH : 0);
        constexpr int issued = issued0 < NCH ? issued0 : NCH;
        wait_vm_bar<(issued - c - PH) * 4>();
        if constexpr (c >= PH)
          static_for<0, PH>([&](auto PI) {
            constexpr int cn = c - PH + RING + decltype(PI)::value;
            if constexpr (cn < NCH) issue(std::integral_constant<int, cn>{});
          });
      }
      const LDSP char* base = (const LDSP char*)smem + slot * SLOT;
#pragma unroll
      for (int s = 0; s < 4; ++s) {
        const u32x4 a0 = *(const LDSP u32x4*)(base + (a_off ^ (s << 5)));
        const u32x4 a1 = *(const LDSP u32x4*)(base + 4096 + (a_off ^ (s << 5)));
        const u32x4 bf = b_frag(base + s * 2048 + b_off);
        if constexpr (VOR) {
          acc[0] = MFMA32(__builtin_bit_cast(bf16x8, a0), __builtin_bit_cast(bf16x8, bf), acc[0]);
          acc[1] = MFMA32(__builtin_bit_cast(bf16x8, a1), __builtin_bit_cast(bf16x8, bf), acc[1]);
        } else {
          acc[0] = MFMA32(__builtin_bit_cast(bf16x8, bf), __builtin_bit_cast(bf16x8, a0), acc[0]);
          acc[1] = MFMA32(__builtin_bit_cast(bf16x8, bf), __builtin_bit_cast(bf16x8, a1), acc[1]);
        }
      }
      if constexpr (QKV) {
#pragma unroll
        for (int i = 0; i < 2; ++i) {
          const u32x4 a = *(const LDSP u32x4*)(base + i * 4096 + (a_off ^ (wn << 5)));
#pragma unroll
          for (int q = 0; q < 4; ++q) {
            s1[i][q & 1] = dot2(a[q], 0x3f803f80u, s1[i][q & 1]);
            s2[i][q & 1] = dot2(a[q], a[q], s2[i][q & 1]);
          }
          const u32x4 bb = b_frag(base + (2 * wm + i) * 2048 + b_off);
#pragma unroll
          for (int q = 0; q < 4; ++q) cs[i] = dot2(bb[q], 0x3f803f80u, cs[i]);
          asm volatile("" : "+v"(s1[i][0]), "+v"(s1[i][1]), "+v"(s2[i][0]), "+v"(s2[i][1]), "+v"(cs[i]));
        }
      }
    });
  };
  const bool isv = QKV && col0 >= 2 * D;
  if (isv) mainloop(std::true_type{});
  else mainloop(std::false_type{});

  if constexpr (QKV) {
#pragma unroll
    for (int i = 0; i < 2; ++i)
      *(f32x2*)(rowp + ((wn * 2 + hh) * 128 + ar + 32 * i) * 2) = f32x2{s1[i][0] + s1[i][1], s2[i][0] + s2[i][1]};
    colp[(wm * 2 + hh) * 128 + wn * 32 + i32] = cs[0] + cs[1];
    __syncthreads();
    if (tid < 128) {
      float a = 0.f, q = 0.f;
#pragma unroll
      for (int p = 0; p < 8; ++p) { f32x2 v = *(const f32x2*)(rowp + (p * 128 + tid) * 2); a += v[0]; q += v[1]; }
      const float mean = a * (1.0f / D);
      const float rs = __builtin_amdgcn_rsqf(fmaxf(q * (1.0f / D) - mean * mean, 0.f) + LN_EPS);
      *(f32x2*)(rowt + tid * 2) = f32x2{rs, rs * mean};
    } else if (tid < 256) {
      const int n = tid - 128;
      colt[n] = colp[n] + colp[128 + n] + colp[256 + n] + colp[384 + n];
    }
    __syncthreads();
    if (isv) {
      const float cn = colt[wn * 32 + i32];
      const int vc = col0 - 2 * D + wn * 32 + i32;
#pragma unroll
      for (int i = 0; i < 2; ++i) {
        u16* dst = g.o2 + (size_t)(vc >> 4) * 32768 + ((row0 >> 5) + wm * 2 + i) * 512 + hh * 128 + (vc & 15) * 8;
#pragma unroll
        for (int s = 0; s < 2; ++s) {
          u32x4 w;
#pragma unroll
          for (int e = 0; e < 4; ++e) {
            const int r0_ = 8 * s + 2 * e;
            const int m = wm * 64 + i * 32 + (r0_ & 3) + 8 * (r0_ >> 2) + 4 * hh;
            const f32x4 rt = *(const f32x4*)(rowt + m * 2);
            w[e] = pk_bf16(__builtin_fmaf(acc[i][r0_], rt[0], -rt[1] * cn), __builtin_fmaf(acc[i][r0_ + 1], rt[2], -rt[3] * cn));
          }
          *(u32x4*)(dst + s * 256) = w;
        }
      }
    } else {
      const float sc_ = col0 < D ? QSCALE : 1.0f;
      u16* dbase = (col0 < D ? g.o0 : g.o1) + (size_t)(((col0 & (D - 1)) + wn * 32) >> 4) * 32768 + i32 * 8 + hh * 4;
#pragma unroll
      for (int i = 0; i < 2; ++i) {
        const f32x2 rt = *(const f32x2*)(rowt + (ar + 32 * i) * 2);
        const float rs = rt[0] * sc_, rm = rt[1] * sc_;
        u16* dst = dbase + ((row0 >> 5) + wm * 2 + i) * 512;
#pragma unroll
        for (int q = 0; q < 4; ++q) {
          const f32x4 cn = *(const f32x4*)(colt + wn * 32 + 8 * q + 4 * hh);
          u32x2 w;
          w[0] = pk_bf16(__builtin_fmaf(acc[i][4 * q], rs, -rm * cn[0]), __builtin_fmaf(acc[i][4 * q + 1], rs, -rm * cn[1]));
          w[1] = pk_bf16(__builtin_fmaf(acc[i][4 * q + 2], rs, -rm * cn[2]), __builtin_fmaf(acc[i][4 * q + 3], rs, -rm * cn[3]));
          *(u32x2*)(dst + (q >> 1) * 32768 + (q & 1) * 256) = w;
        }
      }
    }
  } else {
    bar_lds();
    u16* st = (u16*)smem;
#pragma unroll
    for (int i = 0; i < 2; ++i) {
      const f32x2 rt = *(const f32x2*)(rowt + (ar + 32 * i) * 2);
#pragma unroll
      for (int q = 0; q < 4; ++q) {
        const f32x4 cn = *(const f32x4*)(colt + wn * 32 + 8 * q + 4 * hh);
        float t[4];
#pragma unroll
        for (int e = 0; e < 4; ++e) t[e] = gelu_tanh(rbf(__builtin_fmaf(acc[i][4 * q + e], rt[0], -rt[1] * cn[e])));
        *(u32x2*)(st + (ar + 32 * i) * 136 + wn * 32 + 8 * q + 4 * hh) = u32x2{pk_bf16(t[0], t[1]), pk_bf16(t[2], t[3])};
      }
    }
    __syncthreads();
#pragma unroll
    for (int it = 0; it < 4; ++it) {
      const int item = it * 512 + tid, r = item >> 4, c = (item & 15) * 8;
      u32x4 o = *(const u32x4*)(st + r * 136 + c);
      if (col0 + c >= F) o = u32x4{0u, 0u, 0u, 0u};
      *(u32x4*)(g.o0 + (size_t)(row0 + r) * FP + col0 + c) = o;
    }
  }
}

constexpr int AT_K = 0, AT_V = 65536, AT_C = 131072, AT_LDS = AT_C + 1024 + 2048;

__global__ __launch_bounds__(512) void attn_kernel(const u16* __restrict__ Qf, const u16* __restrict__ Kf,
                                                   const u16* __restrict__ Vf, u16* __restrict__ O,
                                                   const u16* __restrict__ W1, const u16* __restrict__ W3,
                                                   float* __restrict__ csum) {
  extern __shared__ __attribute__((aligned(16))) char smem[];
  const int tid = threadIdx.x, lane = tid & 63, wid = __builtin_amdgcn_readfirstlane(tid >> 6);
  const int b = blockIdx.x, head = (b & 7) * 4 + ((b >> 3) & 3), qb = b >> 5;
  const unsigned lds0 = (unsigned)(uintptr_t)smem;
  const int r32 = lane & 31, hh = lane >> 5;
  const int qt = qb * 8 + wid;

  bf16x8 qf;
  {
    const u16* qp = Qf + (size_t)head * 32768 + qt * 512 + lane * 8;
    asm volatile("global_load_dwordx4 %0, %1, off" : "=v"(qf) : "v"(qp) : "memory");
  }
  const char* kb = (const char*)(Kf + (size_t)head * 32768) + wid * 1024;
  const char* vb = (const char*)(Vf + (size_t)head * 32768) + wid * 1024;
#pragma unroll
  for (int g = 0; g < 8; ++g) glds16(lane * 16, kb + g * 8192, lds0 + AT_K + g * 8192 + wid * 1024);
#pragma unroll
  for (int g = 0; g < 8; ++g) glds16(lane * 16, vb + g * 8192, lds0 + AT_V + g * 8192 + wid * 1024);
  if (tid < 32) {
    const unsigned w = ((tid & 15) == 0) ? 0x3f803f80u : 0u;
    *(LDSP u32x4*)((LDSP char*)smem + AT_C + (tid >> 4) * 512 + (tid & 15) * 16) = u32x4{w, w, w, w};
  }
  const bool is1 = b < 32;
  const int sj = is1 ? b : b - 32, scg = sj >> 2, skq = sj & 3;
  u32x4 wc0, wc1;
  {
    const int ldw = is1 ? D : F;
    int col = scg * 64 + (tid & 7) * 8;
    col = col < ldw - 8 ? col : ldw - 8;
    const u16* wp = (is1 ? W1 : W3) + (size_t)(skq * 128 + (tid >> 3)) * ldw + col;
    asm volatile("global_load_dwordx4 %0, %1, off" : "=v"(wc0) : "v"(wp) : "memory");
    asm volatile("global_load_dwordx4 %0, %1, off" : "=v"(wc1) : "v"(wp + 64 * ldw) : "memory");
  }
  asm volatile("s_waitcnt vmcnt(18)" : "+v"(qf)::"memory");

  const LDSP char* kp = (const LDSP char*)smem + AT_K + lane * 16;
  float mx0 = -3.0e38f, mx1 = -3.0e38f;
  f32x16 zero;
#pragma unroll
  for (int r = 0; r < 16; ++r) zero[r] = 0.f;
#define MX3(a, b, c) __builtin_fmaxf(__builtin_fmaxf((a), (b)), (c))
  static_for<0, 8>([&](auto GI) {
    constexpr int g = decltype(GI)::value;
    wait_vm_bar<17 - g>();
    bf16x8 kf[8];
#pragma unroll
    for (int i = 0; i < 8; ++i) kf[i] = *(const LDSP bf16x8*)(kp + (g * 8 + i) * 1024);
    f32x16 sa = MFMA32(kf[0], qf, zero);
    f32x16 sb = MFMA32(kf[1], qf, zero);
#pragma unroll
    for (int i = 0; i < 8; i += 2) {
      SCHED0();
#pragma unroll
      for (int r = 0; r < 16; r += 4) { mx0 = MX3(mx0, sa[r], sa[r + 1]); mx1 = MX3(mx1, sa[r + 2], sa[r + 3]); }
      if (i + 2 < 8) sa = MFMA32(kf[i + 2], qf, zero);
      SCHED0();
#pragma unroll
      for (int r = 0; r < 16; r += 4) { mx0 = MX3(mx0, sb[r], sb[r + 1]); mx1 = MX3(mx1, sb[r + 2], sb[r + 3]); }
      if (i + 3 < 8) sb = MFMA32(kf[i + 3], qf, zero);
    }
  });
  float mx = __builtin_fmaxf(mx0, mx1);
  {
    auto rr = __builtin_amdgcn_permlane32_swap(__float_as_uint(mx), __float_as_uint(mx), false, false);
    mx = __builtin_fmaxf(__uint_as_float(rr[0]), __uint_as_float(rr[1]));
  }
  f32x16 nm;
#pragma unroll
  for (int r = 0; r < 16; ++r) nm[r] = -mx;

  unsigned vaddr, vinc;
  if (r32 < 16) { vaddr = AT_V + hh * 256 + r32 * 16; vinc = 1024; }
  else { vaddr = AT_C + (r32 == 16 ? 0 : (r32 >= 20 && r32 < 28) ? 64 : 16); vinc = 0; }
  f32x16 o;
#pragma unroll
  for (int r = 0; r < 16; ++r) o[r] = 0.f;
  wait_vm_bar<0>();
  const LDSP char* lb = (const LDSP char*)smem;
  bf16x8 kn = *(const LDSP bf16x8*)(kp);
  f32x16 sA = MFMA32(kn, qf, nm);
  kn = *(const LDSP bf16x8*)(kp + 1024);
  bf16x8 va0 = *(const LDSP bf16x8*)(lb + vaddr), va1 = *(const LDSP bf16x8*)(lb + vaddr + 512);
  f32x16 sB;
  bf16x8 vb0, vb1;
#define EXPK(P, S_, B_)                                                                                       \
  _Pragma("unroll") for (int i = 0; i < 4; ++i)                                                               \
      P[i] = pk_bf16(__builtin_amdgcn_exp2f(S_[B_ + 2 * i]), __builtin_amdgcn_exp2f(S_[B_ + 2 * i + 1]));
#define HALF(SC, SN, VC0, VC1, VN0, VN1, TN)                                                                  \
  {                                                                                                           \
    SN = MFMA32(kn, qf, nm);                                                                                  \
    asm volatile("" : "+v"(SN));                                                                              \
    kn = *(const LDSP bf16x8*)(kp + ((TN) + 1) * 1024);                                                       \
    vaddr += vinc;                                                                                            \
    VN0 = *(const LDSP bf16x8*)(lb + vaddr);                                                                  \
    VN1 = *(const LDSP bf16x8*)(lb + vaddr + 512);                                                            \
    SCHED0();                                                                                                 \
    u32x4 p0, p1;                                                                                             \
    EXPK(p0, SC, 0)                                                                                           \
    o = MFMA32(VC0, __builtin_bit_cast(bf16x8, p0), o);                                                       \
    asm volatile("" : "+v"(o));                                                                               \
    SCHED0();                                                                                                 \
    EXPK(p1, SC, 8)                                                                                           \
    o = MFMA32(VC1, __builtin_bit_cast(bf16x8, p1), o);                                                       \
    asm volatile("" : "+v"(o));                                                                               \
    SCHED0();                                                                                                 \
  }
#pragma unroll 1
  for (int t = 0; t < 62; t += 2) {
    HALF(sA, sB, va0, va1, vb0, vb1, t + 1)
    HALF(sB, sA, vb0, vb1, va0, va1, t + 2)
  }
  {
    sB = MFMA32(kn, qf, nm);
    vaddr += vinc;
    vb0 = *(const LDSP bf16x8*)(lb + vaddr);
    vb1 = *(const LDSP bf16x8*)(lb + vaddr + 512);
    u32x4 p0, p1;
    EXPK(p0, sA, 0)
    o = MFMA32(va0, __builtin_bit_cast(bf16x8, p0), o);
    EXPK(p1, sA, 8)
    o = MFMA32(va1, __builtin_bit_cast(bf16x8, p1), o);
    EXPK(p0, sB, 0)
    o = MFMA32(vb0, __builtin_bit_cast(bf16x8, p0), o);
    EXPK(p1, sB, 8)
    o = MFMA32(vb1, __builtin_bit_cast(bf16x8, p1), o);
  }
  auto rr = __builtin_amdgcn_permlane32_swap(__float_as_uint(o[8]), __float_as_uint(o[8]), false, false);
  const float inv = 1.0f / __uint_as_float(rr[0]);
  u16* op = O + (size_t)(qt * 32 + r32) * D + head * 16 + hh * 4;
  u32x2 w0, w1;
  w0[0] = pk_bf16(o[0] * inv, o[1] * inv); w0[1] = pk_bf16(o[2] * inv, o[3] * inv);
  w1[0] = pk_bf16(o[4] * inv, o[5] * inv); w1[1] = pk_bf16(o[6] * inv, o[7] * inv);
  *(u32x2*)op = w0;
  *(u32x2*)(op + 8) = w1;

  if (b < 32 + 4 * (FP / 64)) {
    asm volatile("" : "+v"(wc0), "+v"(wc1));
    float cv[8];
#pragma unroll
    for (int i = 0; i < 4; ++i) {
      cv[2 * i] = bf_lo(wc0[i]) + bf_lo(wc1[i]);
      cv[2 * i + 1] = bf_hi(wc0[i]) + bf_hi(wc1[i]);
    }
#pragma unroll
    for (int i = 0; i < 8; ++i) {
      float t = dpp_add<0x128>(cv[i]);
      auto a = __builtin_amdgcn_permlane16_swap(__float_as_uint(t), __float_as_uint(t), false, false);
      t = __uint_as_float(a[0]) + __uint_as_float(a[1]);
      auto c = __builtin_amdgcn_permlane32_swap(__float_as_uint(t), __float_as_uint(t), false, false);
      cv[i] = __uint_as_float(c[0]) + __uint_as_float(c[1]);
    }
    float* red = (float*)(smem + AT_C + 1024);
    if (lane < 8) {
      *(f32x4*)(red + wid * 64 + lane * 8) = f32x4{cv[0], cv[1], cv[2], cv[3]};
      *(f32x4*)(red + wid * 64 + lane * 8 + 4) = f32x4{cv[4], cv[5], cv[6], cv[7]};
    }
    __syncthreads();
    if (tid < 64) {
      float t = 0.f;
#pragma unroll
      for (int i = 0; i < 8; ++i) t += red[i * 64 + tid];
      const int col = scg * 64 + tid;
      if (!is1 && col >= F) t = 0.f;
      csum[skq * (D + FP) + (is1 ? 0 : D) + col] = t;
    }
  }
}

extern "C" void kernel_launch(void* const* d_in, const int* in_sizes, int n_in, void* d_out, int out_size, void* d_ws,
                              size_t ws_size, hipStream_t stream) {
  const u16* x = (const u16*)d_in[0];
  const u16* wqkv = (const u16*)d_in[1];
  const u16* wo = (const u16*)d_in[2];
  const u16* w1 = (const u16*)d_in[4];
  const u16* w2 = (const u16*)d_in[5];
  const u16* w3 = (const u16*)d_in[6];
  const u16* w4 = (const u16*)d_in[7];
  u16* out = (u16*)d_out;
  char* ws = (char*)d_ws;
  const size_t MB = 1u << 20;
  u16* q = (u16*)(ws + 0 * MB);
  u16* k = (u16*)(ws + 2 * MB);
  u16* v = (u16*)(ws + 4 * MB);
  u16* o = (u16*)(ws + 6 * MB);
  u16* h = (u16*)(ws + 8 * MB);
  float* stats = (float*)(ws + 10 * MB);
  u16* g = (u16*)(ws + 12 * MB);
  u16* x1 = (u16*)(ws + 15 * MB);
  u16* u = (u16*)(ws + 17 * MB);

  float* part = (float*)(ws + 10 * MB + 65536);
  float* csum = (float*)(ws + 11 * MB);
  constexpr int RN = 8, LDSN = RN * 2 * CH;
  auto k_qkv = gemm_w<true, 3 * D>;
  auto k_wo = gemm_n<8, RN, 2, D, D, EPI_WO>;
  auto k_w1 = gemm_n<8, RN, 2, D, D, EPI_W1>;
  auto k_w2 = gemm_n<8, RN, 2, D, D, EPI_W2>;
  auto k_w3 = gemm_w<false, F>;
  auto k_w4 = gemm_n<FP / 64, RN, 2, FP, F, EPI_W4>;
  (void)hipFuncSetAttribute((const void*)k_qkv, hipFuncAttributeMaxDynamicSharedMemorySize, W_LDS);
  (void)hipFuncSetAttribute((const void*)k_wo, hipFuncAttributeMaxDynamicSharedMemorySize, LDSN);
  (void)hipFuncSetAttribute((const void*)k_w1, hipFuncAttributeMaxDynamicSharedMemorySize, LDSN);
  (void)hipFuncSetAttribute((const void*)k_w2, hipFuncAttributeMaxDynamicSharedMemorySize, LDSN);
  (void)hipFuncSetAttribute((const void*)k_w3, hipFuncAttributeMaxDynamicSharedMemorySize, W_LDS);
  (void)hipFuncSetAttribute((const void*)k_w4, hipFuncAttributeMaxDynamicSharedMemorySize, LDSN);
  (void)hipFuncSetAttribute((const void*)attn_kernel, hipFuncAttributeMaxDynamicSharedMemorySize, AT_LDS);

  k_qkv<<<16 * 12, 512, W_LDS, stream>>>(GArgs{x, wqkv, nullptr, nullptr, q, k, v, nullptr, nullptr, nullptr});
  attn_kernel<<<256, 512, AT_LDS, stream>>>(q, k, v, o, w1, w3, csum);
  k_wo<<<256, 512, LDSN, stream>>>(GArgs{o, wo, x, nullptr, h, nullptr, nullptr, nullptr, part, nullptr});
  k_w1<<<256, 512, LDSN, stream>>>(GArgs{h, w1, nullptr, nullptr, g, nullptr, nullptr, stats, part, csum});
  k_w2<<<256, 512, LDSN, stream>>>(GArgs{g, w2, h, nullptr, x1, nullptr, nullptr, stats, part, nullptr});
  k_w3<<<16 * 11, 512, W_LDS, stream>>>(GArgs{x1, w3, nullptr, nullptr, u, nullptr, nullptr, nullptr, part, csum + D});
  k_w4<<<256, 512, LDSN, stream>>>(GArgs{u, w4, h, x1, out, nullptr, nullptr, nullptr, nullptr, nullptr});
}
```

```cpp
#include <hip/hip_runtime.h>
#include <hip/hip_bf16.h>
#include <stdint.h>

typedef __attribute__((ext_vector_type(8))) short bf16x8;
typedef __attribute__((ext_vector_type(4))) short s16x4;
typedef __attribute__((ext_vector_type(16))) float f32x16;
typedef __attribute__((ext_vector_type(4))) float f32x4;
typedef __attribute__((ext_vector_type(2))) float f32x2;
typedef __attribute__((ext_vector_type(2))) __bf16 bf16x2_t;
typedef __attribute__((ext_vector_type(4))) unsigned u32x4;
typedef __attribute__((ext_vector_type(2))) unsigned u32x2;
typedef unsigned short u16;

#define DI __device__ __forceinline__
#define LDSP __attribute__((address_space(3)))
#define MFMA32(a, b, c) __builtin_amdgcn_mfma_f32_32x32x16_bf16((a), (b), (c), 0, 0, 0)
#define SCHED0() __builtin_amdgcn_sched_barrier(0)

constexpr int S = 2048, D = 512, NH = 32, F = 1376, FP = 1408;
constexpr float LN_EPS = 1e-5f;
constexpr float QSCALE = 0.25f * 1.4426950408889634f;

DI unsigned pk_bf16(float a, float b) {
  f32x2 v = {a, b};
  bf16x2_t r = __builtin_convertvector(v, bf16x2_t);
  return __builtin_bit_cast(unsigned, r);
}
DI float bf_lo(unsigned u) { return __uint_as_float(u << 16); }
DI float bf_hi(unsigned u) { return __uint_as_float(u & 0xffff0000u); }
DI float rbf(float x) { return bf_lo(pk_bf16(x, x)); }
DI float dot2(unsigned a, unsigned b, float c) {
  return __builtin_amdgcn_fdot2_f32_bf16(__builtin_bit_cast(bf16x2_t, a), __builtin_bit_cast(bf16x2_t, b), c, false);
}
template <int CTRL> DI float dpp_add(float v) {
  int r = __builtin_amdgcn_update_dpp(0, __float_as_int(v), CTRL, 0xf, 0xf, true);
  return v + __int_as_float(r);
}
template <int CTRL> DI float dpp_max(float v) {
  int r = __builtin_amdgcn_update_dpp(__float_as_int(v), __float_as_int(v), CTRL, 0xf, 0xf, false);
  return __builtin_fmaxf(v, __int_as_float(r));
}
DI float half_sum(float v) {
  auto r = __builtin_amdgcn_permlane32_swap(__float_as_uint(v), __float_as_uint(v), false, false);
  return __uint_as_float(r[0]) + __uint_as_float(r[1]);
}
DI float max32(float v) {
  v = dpp_max<0xB1>(v); v = dpp_max<0x4E>(v); v = dpp_max<0x141>(v); v = dpp_max<0x140>(v);
  auto r = __builtin_amdgcn_permlane16_swap(__float_as_uint(v), __float_as_uint(v), false, false);
  return __builtin_fmaxf(__uint_as_float(r[0]), __uint_as_float(r[1]));
}
DI float gelu_tanh(float x) {
  const float c1 = -2.0f * 0.7978845608028654f * 1.4426950408889634f;
  const float c3 = c1 * 0.044715f;
  float t = x * __builtin_fmaf(c3, x * x, c1);
  float e = __builtin_amdgcn_exp2f(t);
  return x * __builtin_amdgcn_rcpf(1.0f + e);
}

DI void glds16(unsigned voff, const void* sbase, unsigned lds_dst) {
  unsigned keep;
  asm volatile("s_mov_b32 %0, m0\n\ts_mov_b32 m0, %3\n\ts_nop 0\n\tglobal_load_lds_dwordx4 %1, %2\n\ts_mov_b32 m0, %0"
               : "=&s"(keep) : "v"(voff), "s"(sbase), "s"(lds_dst) : "memory");
}
DI void glds16v(const void* vptr, unsigned lds_dst) {
  unsigned keep;
  asm volatile("s_mov_b32 %0, m0\n\ts_mov_b32 m0, %2\n\ts_nop 0\n\tglobal_load_lds_dwordx4 %1, off\n\ts_mov_b32 m0, %0"
               : "=&s"(keep) : "v"(vptr), "s"(lds_dst) : "memory");
}
template <int N> DI void wait_vm_bar() {
  asm volatile("s_waitcnt vmcnt(%0) lgkmcnt(0)\n\ts_barrier" ::"i"(N) : "memory");
}
DI void bar_lds() { asm volatile("s_waitcnt lgkmcnt(0)\n\ts_barrier" ::: "memory"); }
template <int I, int N, class Fn> DI void static_for(Fn&& f) {
  if constexpr (I < N) {
    f(std::integral_constant<int, I>{});
    static_for<I + 1, N>(f);
  }
}
DI s16x4 tr_read(const LDSP char* p) {
  typedef short v4i16_t __attribute__((ext_vector_type(4)));
  return __builtin_bit_cast(s16x4, __builtin_amdgcn_ds_read_tr16_b64_v4i16((LDSP v4i16_t*)p));
}

constexpr int CH = 8192;
enum { EPI_WO = 1, EPI_W1 = 2, EPI_W2 = 3, EPI_W4 = 5 };

struct GArgs {
  const u16* A;
  const u16* W;
  const u16* r0;
  const u16* r1;
  u16* o0;
  u16* o1;
  u16* o2;
  float* stats;
  float* part;
  const float* csum;
};

DI unsigned b_lane_off(int lane) {
  const int hh = lane >> 5, blk = (lane >> 4) & 1, q = (lane & 15) >> 2, p = lane & 3;
  const int kr = 8 * hh + q;
  return kr * 128 + (((2 * blk + (p >> 1)) ^ (((kr >> 1) & 1) << 2)) << 4) + 8 * (p & 1);
}
DI u32x4 b_frag(const LDSP char* bp) {
  const u32x2 lo = __builtin_bit_cast(u32x2, tr_read(bp));
  const u32x2 hi = __builtin_bit_cast(u32x2, tr_read(bp + 512));
  return u32x4{lo[0], lo[1], hi[0], hi[1]};
}
DI f32x2 row8_stats(const u32x4& o) {
  float a = 0.f, q = 0.f;
#pragma unroll
  for (int i = 0; i < 4; ++i) { a = dot2(o[i], 0x3f803f80u, a); q = dot2(o[i], o[i], q); }
  a = dpp_add<0xB1>(a); q = dpp_add<0xB1>(q);
  a = dpp_add<0x4E>(a); q = dpp_add<0x4E>(q);
  a = dpp_add<0x141>(a); q = dpp_add<0x141>(q);
  return f32x2{a, q};
}

template <int NCH, int RING, int PH, int LDA, int KW, int EPI>
__global__ __launch_bounds__(512) void gemm_n(GArgs g) {
  extern __shared__ __attribute__((aligned(16))) char smem[];
  constexpr bool LN = (EPI == EPI_W1);
  constexpr bool SHIFT = (EPI == EPI_W2);
  constexpr int SLOT = 2 * CH, LDW = D;
  const int tid = threadIdx.x, lane = tid & 63, wid = __builtin_amdgcn_readfirstlane(tid >> 6);
  const int b = blockIdx.x, rb = (b & 7) * 4 + ((b >> 3) & 3), cb = b >> 5;
  const int row0 = rb * 64, col0 = cb * 64;
  const unsigned lds0 = (unsigned)(uintptr_t)smem;

  const int er = tid >> 3, ec = (tid & 7) * 8;
  const size_t eo = (size_t)(row0 + er) * D + col0 + ec;
  u32x4 rv0, rv1;
  f32x2 rst;
  f32x4 cv0, cv1;
  if constexpr (EPI == EPI_WO || EPI == EPI_W2 || EPI == EPI_W4) rv0 = *(const u32x4*)(g.r0 + eo);
  if constexpr (EPI == EPI_W4) rv1 = *(const u32x4*)(g.r1 + eo);
  if constexpr (EPI == EPI_W2) rst = *(const f32x2*)(g.stats + (row0 + er) * 2);
  if constexpr (LN) {
    rst = *(const f32x2*)(g.part + ((row0 + er) * 8 + (tid & 7)) * 2);
    const float* cp = g.csum + col0 + ec;
    cv0 = (*(const f32x4*)(cp) + *(const f32x4*)(cp + (D + FP))) +
          (*(const f32x4*)(cp + 2 * (D + FP)) + *(const f32x4*)(cp + 3 * (D + FP)));
    cv1 = (*(const f32x4*)(cp + 4) + *(const f32x4*)(cp + 4 + (D + FP))) +
          (*(const f32x4*)(cp + 4 + 2 * (D + FP)) + *(const f32x4*)(cp + 4 + 3 * (D + FP)));
  }

  const int sr = wid * 8 + (lane >> 3), sc = lane & 7;
  const unsigned aoff = (unsigned)(sr * LDA + ((sc ^ ((sr >> 1) & 7)) << 3)) * 2u;
  const unsigned boff = (unsigned)(col0 + ((sc ^ (((sr >> 1) & 1) << 2)) << 3)) * 2u;
  const char* Ab = (const char*)(g.A + (size_t)row0 * LDA);
  auto issue = [&](auto CI) {
    constexpr int c = decltype(CI)::value, slot = c % RING;
    constexpr int sh = SHIFT ? ((0x4031 >> (((c >> 1) & 3) * 4)) & 15) : 0;
    const unsigned dst = lds0 + slot * SLOT + wid * 1024;
    glds16(aoff, Ab + (size_t)sh * (LDA * 2) + c * 128, dst);
    int kr = c * 64 + sr;
    if ((c + 1) * 64 > KW) kr = kr < KW ? kr : KW - 1;
    glds16(boff + (unsigned)kr * (LDW * 2), g.W, dst + CH);
  };
  constexpr int PRE = RING < NCH ? RING : NCH;
  static_for<0, PRE>(issue);
  const int kg = wid >> 2, wm = (wid >> 1) & 1, wn = wid & 1;
  const int i32 = lane & 31, hh = lane >> 5;
  const int ar = wm * 32 + i32;
  const unsigned a_off = ar * 128 + (((4 * kg + hh) ^ ((ar >> 1) & 7)) << 4);
  const unsigned b_off = CH + kg * 4096 + (b_lane_off(lane) ^ (wn << 6));
  f32x16 acc;
#pragma unroll
  for (int r = 0; r < 16; ++r) acc[r] = 0.f;
  static_assert(RING % PH == 0 && RING >= 2 * PH && NCH % PH == 0, "ring / phase geometry");
  static_for<0, NCH>([&](auto CI) {
    constexpr int c = decltype(CI)::value, slot = c % RING;
    if constexpr (c % PH == 0) {
      constexpr int issued0 = RING + (c >= PH ? c - PH : 0);
      constexpr int issued = issued0 < NCH ? issued0 : NCH;
      wait_vm_bar<(issued - c - PH) * 2>();
      if constexpr (c >= PH)
        static_for<0, PH>([&](auto PI) {
          constexpr int cn = c - PH + RING + decltype(PI)::value;
          if constexpr (cn < NCH) issue(std::integral_constant<int, cn>{});
        });
    }
    const LDSP char* base = (const LDSP char*)smem + slot * SLOT;
#pragma unroll
    for (int s = 0; s < 2; ++s) {
      const u32x4 af = *(const LDSP u32x4*)(base + (a_off ^ (s << 5)));
      const u32x4 bf = b_frag(base + s * 2048 + b_off);
      acc = MFMA32(__builtin_bit_cast(bf16x8, bf), __builtin_bit_cast(bf16x8, af), acc);
    }
  });
  bar_lds();

  constexpr int LDS_ = 68;
  float* st = (float*)smem;
#pragma unroll
  for (int q = 0; q < 4; ++q) {
    f32x4 v = {acc[4 * q], acc[4 * q + 1], acc[4 * q + 2], acc[4 * q + 3]};
    *(f32x4*)(st + (kg * 64 + ar) * LDS_ + wn * 32 + 8 * q + 4 * hh) = v;
  }
  __syncthreads();
  float v[8];
  {
    const f32x4 a0 = *(const f32x4*)(st + er * LDS_ + ec), a1 = *(const f32x4*)(st + er * LDS_ + ec + 4);
    const f32x4 b0 = *(const f32x4*)(st + (64 + er) * LDS_ + ec), b1 = *(const f32x4*)(st + (64 + er) * LDS_ + ec + 4);
#pragma unroll
    for (int i = 0; i < 4; ++i) { v[i] = a0[i] + b0[i]; v[4 + i] = a1[i] + b1[i]; }
  }
  u32x4 o;
  if constexpr (EPI == EPI_WO) {
#pragma unroll
    for (int i = 0; i < 4; ++i) o[i] = pk_bf16(bf_lo(rv0[i]) + rbf(v[2 * i]), bf_hi(rv0[i]) + rbf(v[2 * i + 1]));
    *(u32x4*)(g.o0 + eo) = o;
    const f32x2 ps = row8_stats(o);
    if ((tid & 7) == 0) *(f32x2*)(g.part + ((row0 + er) * 8 + cb) * 2) = ps;
  } else if constexpr (EPI == EPI_W1) {
    float a = rst[0], q = rst[1];
    a = dpp_add<0xB1>(a); q = dpp_add<0xB1>(q);
    a = dpp_add<0x4E>(a); q = dpp_add<0x4E>(q);
    a = dpp_add<0x141>(a); q = dpp_add<0x141>(q);
    const float mean = a * (1.0f / D);
    const float var = fmaxf(q * (1.0f / D) - mean * mean, 0.f);
    float rs = __builtin_amdgcn_rsqf(var + LN_EPS);
    if (cb == 0 && (tid & 7) == 0) *(f32x2*)(g.stats + (row0 + er) * 2) = f32x2{mean, rs};
    rs *= __builtin_amdgcn_rsqf(var * rs * rs + LN_EPS);
    const float rm = rs * mean;
#pragma unroll
    for (int i = 0; i < 4; ++i) {
      v[i] = __builtin_fmaf(v[i], rs, -rm * cv0[i]);
      v[4 + i] = __builtin_fmaf(v[4 + i], rs, -rm * cv1[i]);
    }
#pragma unroll
    for (int i = 0; i < 4; ++i) o[i] = pk_bf16(gelu_tanh(rbf(v[2 * i])), gelu_tanh(rbf(v[2 * i + 1])));
    *(u32x4*)(g.o0 + eo + 2 * D) = o;
    if ((rb == 0 || rb == 31) && tid < 16) {
      const int hr = (rb == 0 ? 0 : S + 2) + (tid >> 3);
      *(u32x4*)(g.o0 + (size_t)hr * D + col0 + ec) = u32x4{0u, 0u, 0u, 0u};
    }
  } else if constexpr (EPI == EPI_W2) {
#pragma unroll
    for (int i = 0; i < 4; ++i) {
      const float xl = rbf((bf_lo(rv0[i]) - rst[0]) * rst[1]), xh = rbf((bf_hi(rv0[i]) - rst[0]) * rst[1]);
      o[i] = pk_bf16(xl + rbf(v[2 * i]), xh + rbf(v[2 * i + 1]));
    }
    *(u32x4*)(g.o0 + eo) = o;
    const f32x2 ps = row8_stats(o);
    if ((tid & 7) == 0) *(f32x2*)(g.part + ((row0 + er) * 8 + cb) * 2) = ps;
  } else {
#pragma unroll
    for (int i = 0; i < 4; ++i)
      o[i] = pk_bf16(bf_lo(rv0[i]) + rbf(bf_lo(rv1[i]) + rbf(v[2 * i])),
                     bf_hi(rv0[i]) + rbf(bf_hi(rv1[i]) + rbf(v[2 * i + 1])));
    *(u32x4*)(g.o0 + eo) = o;
  }
}

constexpr int W_RING = 4, W_SLOT = 4 * CH, W_TAB = W_RING * W_SLOT;
constexpr int W_LDS = W_TAB + 16384;

template <bool QKV, int LDW>
__global__ __launch_bounds__(512) void gemm_w(GArgs g) {
  extern __shared__ __attribute__((aligned(16))) char smem[];
  constexpr int NCH = 8, RING = W_RING, PH = 2, SLOT = W_SLOT;
  const int tid = threadIdx.x, lane = tid & 63, wid = __builtin_amdgcn_readfirstlane(tid >> 6);
  const int b = blockIdx.x, rb = (b & 7) * 2 + ((b >> 3) & 1), cb = b >> 4;
  const int row0 = rb * 128, col0 = cb * 128;
  const unsigned lds0 = (unsigned)(uintptr_t)smem;
  float* rowt = (float*)(smem + W_TAB);
  float* colt = rowt + 256;
  float* rowp = colt + 128;
  float* colp = rowp + 2048;

  f32x4 pv[4];
  float cvv[4];
  if constexpr (!QKV) {
    const float* pp = g.part + (row0 + (tid & 127)) * 16;
    const float* cp = g.csum + col0 + (tid & 127);
#pragma unroll
    for (int i = 0; i < 4; ++i)
      asm volatile("global_load_dwordx4 %0, %1, off offset:%2" : "=v"(pv[i]) : "v"(pp), "i"(16 * i) : "memory");
#pragma unroll
    for (int i = 0; i < 4; ++i)
      asm volatile("global_load_dword %0, %1, off" : "=v"(cvv[i]) : "v"(cp + i * (D + FP)) : "memory");
  }
  const int sr = wid * 8 + (lane >> 3), sc = lane & 7;
  const unsigned aoff = (unsigned)(sr * D + ((sc ^ ((sr >> 1) & 7)) << 3)) * 2u;
  unsigned boff[2];
#pragma unroll
  for (int st = 0; st < 2; ++st) {
    int col = col0 + st * 64 + ((sc ^ (((sr >> 1) & 1) << 2)) << 3);
    if (!QKV) col = col < LDW - 8 ? col : LDW - 8;
    boff[st] = (unsigned)(sr * LDW + col) * 2u;
  }
  const char* Ab = (const char*)(g.A + (size_t)row0 * D);
  auto issue = [&](auto CI) {
    constexpr int c = decltype(CI)::value, slot = c % RING;
    const unsigned dst = lds0 + slot * SLOT + wid * 1024;
    glds16(aoff, Ab + c * 128, dst);
    glds16(aoff, Ab + 64 * D * 2 + c * 128, dst + CH);
    const char* wb = (const char*)g.W + (size_t)c * 64 * LDW * 2;
    glds16(boff[0], wb, dst + 2 * CH);
    glds16(boff[1], wb, dst + 3 * CH);
  };
  static_for<0, RING>(issue);

  if constexpr (!QKV) {
    asm volatile("s_waitcnt vmcnt(16)" : "+v"(pv[0]), "+v"(pv[1]), "+v"(pv[2]), "+v"(pv[3]),
                 "+v"(cvv[0]), "+v"(cvv[1]), "+v"(cvv[2]), "+v"(cvv[3])::"memory");
    if (tid < 128) {
      const float a = (pv[0][0] + pv[0][2]) + (pv[1][0] + pv[1][2]) + (pv[2][0] + pv[2][2]) + (pv[3][0] + pv[3][2]);
      const float q = (pv[0][1] + pv[0][3]) + (pv[1][1] + pv[1][3]) + (pv[2][1] + pv[2][3]) + (pv[3][1] + pv[3][3]);
      const float mean = a * (1.0f / D);
      const float rs = __builtin_amdgcn_rsqf(fmaxf(q * (1.0f / D) - mean * mean, 0.f) + LN_EPS);
      *(f32x2*)(rowt + tid * 2) = f32x2{rs, rs * mean};
    } else if (tid < 256) {
      colt[tid - 128] = (cvv[0] + cvv[1]) + (cvv[2] + cvv[3]);
    }
  }

  const int wm = wid >> 2, wn = wid & 3;
  const int i32 = lane & 31, hh = lane >> 5;
  const int ar = wm * 64 + i32;
  const unsigned a_off = ar * 128 + ((hh ^ ((ar >> 1) & 7)) << 4);
  const unsigned b_off = 2 * CH + (wn >> 1) * CH + (b_lane_off(lane) ^ ((wn & 1) << 6));
  f32x16 acc[2];
  float s1[2][2], s2[2][2], cs[2];
#pragma unroll
  for (int i = 0; i < 2; ++i) {
    s1[i][0] = s1[i][1] = s2[i][0] = s2[i][1] = cs[i] = 0.f;
#pragma unroll
    for (int r = 0; r < 16; ++r) acc[i][r] = 0.f;
  }
  auto mainloop = [&](auto VO) {
    constexpr bool VOR = decltype(VO)::value;
    static_for<0, NCH>([&](auto CI) {
      constexpr int c = decltype(CI)::value, slot = c % RING;
      if constexpr (c % PH == 0) {
        constexpr int issued0 = RING + (c >= PH ? c - PH : 0);
        constexpr int issued = issued0 < NCH ? issued0 : NCH;
        wait_vm_bar<(issued - c - PH) * 4>();
        if constexpr (c >= PH)
          static_for<0, PH>([&](auto PI) {
            constexpr int cn = c - PH + RING + decltype(PI)::value;
            if constexpr (cn < NCH) issue(std::integral_constant<int, cn>{});
          });
      }
      const LDSP char* base = (const LDSP char*)smem + slot * SLOT;
#pragma unroll
      for (int s = 0; s < 4; ++s) {
        const u32x4 a0 = *(const LDSP u32x4*)(base + (a_off ^ (s << 5)));
        const u32x4 a1 = *(const LDSP u32x4*)(base + 4096 + (a_off ^ (s << 5)));
        const u32x4 bf = b_frag(base + s * 2048 + b_off);
        if constexpr (VOR) {
          acc[0] = MFMA32(__builtin_bit_cast(bf16x8, a0), __builtin_bit_cast(bf16x8, bf), acc[0]);
          acc[1] = MFMA32(__builtin_bit_cast(bf16x8, a1), __builtin_bit_cast(bf16x8, bf), acc[1]);
        } else {
          acc[0] = MFMA32(__builtin_bit_cast(bf16x8, bf), __builtin_bit_cast(bf16x8, a0), acc[0]);
          acc[1] = MFMA32(__builtin_bit_cast(bf16x8, bf), __builtin_bit_cast(bf16x8, a1), acc[1]);
        }
      }
      if constexpr (QKV) {
#pragma unroll
        for (int i = 0; i < 2; ++i) {
          const u32x4 a = *(const LDSP u32x4*)(base + i * 4096 + (a_off ^ (wn << 5)));
#pragma unroll
          for (int q = 0; q < 4; ++q) {
            s1[i][q & 1] = dot2(a[q], 0x3f803f80u, s1[i][q & 1]);
            s2[i][q & 1] = dot2(a[q], a[q], s2[i][q & 1]);
          }
          const u32x4 bb = b_frag(base + (2 * wm + i) * 2048 + b_off);
#pragma unroll
          for (int q = 0; q < 4; ++q) cs[i] = dot2(bb[q], 0x3f803f80u, cs[i]);
          asm volatile("" : "+v"(s1[i][0]), "+v"(s1[i][1]), "+v"(s2[i][0]), "+v"(s2[i][1]), "+v"(cs[i]));
        }
      }
    });
  };
  const bool isv = QKV && col0 >= 2 * D;
  if (isv) mainloop(std::true_type{});
  else mainloop(std::false_type{});

  if constexpr (QKV) {
#pragma unroll
    for (int i = 0; i < 2; ++i)
      *(f32x2*)(rowp + ((wn * 2 + hh) * 128 + ar + 32 * i) * 2) = f32x2{s1[i][0] + s1[i][1], s2[i][0] + s2[i][1]};
    colp[(wm * 2 + hh) * 128 + wn * 32 + i32] = cs[0] + cs[1];
    __syncthreads();
    if (tid < 128) {
      float a = 0.f, q = 0.f;
#pragma unroll
      for (int p = 0; p < 8; ++p) { f32x2 v = *(const f32x2*)(rowp + (p * 128 + tid) * 2); a += v[0]; q += v[1]; }
      const float mean = a * (1.0f / D);
      const float rs = __builtin_amdgcn_rsqf(fmaxf(q * (1.0f / D) - mean * mean, 0.f) + LN_EPS);
      *(f32x2*)(rowt + tid * 2) = f32x2{rs, rs * mean};
    } else if (tid < 256) {
      const int n = tid - 128;
      colt[n] = colp[n] + colp[128 + n] + colp[256 + n] + colp[384 + n];
    }
    __syncthreads();
    if (isv) {
      const float cn = colt[wn * 32 + i32];
      const int vc = col0 - 2 * D + wn * 32 + i32;
#pragma unroll
      for (int i = 0; i < 2; ++i) {
        u16* dst = g.o2 + (size_t)(vc >> 4) * 32768 + ((row0 >> 5) + wm * 2 + i) * 512 + hh * 128 + (vc & 15) * 8;
#pragma unroll
        for (int s = 0; s < 2; ++s) {
          u32x4 w;
#pragma unroll
          for (int e = 0; e < 4; ++e) {
            const int r0_ = 8 * s + 2 * e;
            const int m = wm * 64 + i * 32 + (r0_ & 3) + 8 * (r0_ >> 2) + 4 * hh;
            const f32x4 rt = *(const f32x4*)(rowt + m * 2);
            w[e] = pk_bf16(__builtin_fmaf(acc[i][r0_], rt[0], -rt[1] * cn), __builtin_fmaf(acc[i][r0_ + 1], rt[2], -rt[3] * cn));
          }
          *(u32x4*)(dst + s * 256) = w;
        }
      }
    } else {
      const float sc_ = col0 < D ? QSCALE : 1.0f;
      u16* dbase = (col0 < D ? g.o0 : g.o1) + (size_t)(((col0 & (D - 1)) + wn * 32) >> 4) * 32768 + i32 * 8 + hh * 4;
#pragma unroll
      for (int i = 0; i < 2; ++i) {
        const f32x2 rt = *(const f32x2*)(rowt + (ar + 32 * i) * 2);
        const float rs = rt[0] * sc_, rm = rt[1] * sc_;
        u16* dst = dbase + ((row0 >> 5) + wm * 2 + i) * 512;
        float ss[2] = {0.f, 0.f};
#pragma unroll
        for (int q = 0; q < 4; ++q) {
          const f32x4 cn = *(const f32x4*)(colt + wn * 32 + 8 * q + 4 * hh);
          u32x2 w;
          w[0] = pk_bf16(__builtin_fmaf(acc[i][4 * q], rs, -rm * cn[0]), __builtin_fmaf(acc[i][4 * q + 1], rs, -rm * cn[1]));
          w[1] = pk_bf16(__builtin_fmaf(acc[i][4 * q + 2], rs, -rm * cn[2]), __builtin_fmaf(acc[i][4 * q + 3], rs, -rm * cn[3]));
          *(u32x2*)(dst + (q >> 1) * 32768 + (q & 1) * 256) = w;
          ss[q >> 1] = dot2(w[0], w[0], dot2(w[1], w[1], ss[q >> 1]));
        }
        if (col0 >= D) {
          const float m0 = max32(half_sum(ss[0])), m1 = max32(half_sum(ss[1]));
          if (lane == 0) {
            float* km = g.stats + (((col0 - D) + wn * 32) >> 4) * 64 + (row0 >> 5) + wm * 2 + i;
            km[0] = m0;
            km[64] = m1;
          }
        }
      }
    }
  } else {
    bar_lds();
    u16* st = (u16*)smem;
#pragma unroll
    for (int i = 0; i < 2; ++i) {
      const f32x2 rt = *(const f32x2*)(rowt + (ar + 32 * i) * 2);
#pragma unroll
      for (int q = 0; q < 4; ++q) {
        const f32x4 cn = *(const f32x4*)(colt + wn * 32 + 8 * q + 4 * hh);
        float t[4];
#pragma unroll
        for (int e = 0; e < 4; ++e) t[e] = gelu_tanh(rbf(__builtin_fmaf(acc[i][4 * q + e], rt[0], -rt[1] * cn[e])));
        *(u32x2*)(st + (ar + 32 * i) * 136 + wn * 32 + 8 * q + 4 * hh) = u32x2{pk_bf16(t[0], t[1]), pk_bf16(t[2], t[3])};
      }
    }
    __syncthreads();
#pragma unroll
    for (int it = 0; it < 4; ++it) {
      const int item = it * 512 + tid, r = item >> 4, c = (item & 15) * 8;
      u32x4 o = *(const u32x4*)(st + r * 136 + c);
      if (col0 + c >= F) o = u32x4{0u, 0u, 0u, 0u};
      *(u32x4*)(g.o0 + (size_t)(row0 + r) * FP + col0 + c) = o;
    }
  }
}

#define ATT_THR 8.0f
constexpr int AT_K = 0, AT_V = 65536, AT_C = 131072, AT_PF = AT_C + 1024 + 2048, AT_SJ = AT_PF + 8192,
              AT_LDS = AT_SJ + 16384;

__global__ __launch_bounds__(512) void attn_kernel(const u16* __restrict__ Qf, const u16* __restrict__ Kf,
                                                   const u16* __restrict__ Vf, u16* __restrict__ O,
                                                   const u16* __restrict__ W1, const u16* __restrict__ W3,
                                                   float* __restrict__ csum, const u16* __restrict__ Wo,
                                                   const u16* __restrict__ W2, const u16* __restrict__ W4,
                                                   const float* __restrict__ kmax) {
  extern __shared__ __attribute__((aligned(16))) char smem[];
  const int tid = threadIdx.x, lane = tid & 63, wid = __builtin_amdgcn_readfirstlane(tid >> 6);
  const int b = blockIdx.x, head = (b & 7) * 4 + ((b >> 3) & 3), qb = b >> 5;
  const unsigned lds0 = (unsigned)(uintptr_t)smem;
  const int r32 = lane & 31, hh = lane >> 5;
  const int qt = qb * 8 + wid;

  bf16x8 qf;
  {
    const u16* qp = Qf + (size_t)head * 32768 + qt * 512 + lane * 8;
    asm volatile("global_load_dwordx4 %0, %1, off" : "=v"(qf) : "v"(qp) : "memory");
  }
  float kmx;
  asm volatile("global_load_dword %0, %1, off" : "=v"(kmx) : "v"(kmax + head * 64 + lane) : "memory");
  const char* kb = (const char*)(Kf + (size_t)head * 32768) + wid * 1024;
  const char* vb = (const char*)(Vf + (size_t)head * 32768) + wid * 1024;
  auto load_kv = [&](int g) {
    glds16(lane * 16, kb + g * 8192, lds0 + AT_K + g * 8192 + wid * 1024);
    glds16(lane * 16, vb + g * 8192, lds0 + AT_V + g * 8192 + wid * 1024);
  };
  load_kv(0);
  load_kv(1);
  if (tid < 32) {
    const unsigned w = ((tid & 15) == 0) ? 0x3f803f80u : 0u;
    *(LDSP u32x4*)((LDSP char*)smem + AT_C + (tid >> 4) * 512 + (tid & 15) * 16) = u32x4{w, w, w, w};
  }
  const bool is1 = b < 32;
  const int sj = is1 ? b : b - 32, scg = sj >> 2, skq = sj & 3;
  auto load_side = [&](int i) {
    if (i < 2) {
      const int ldw = is1 ? D : F;
      int col = scg * 64 + (tid & 7) * 8;
      col = col < ldw - 8 ? col : ldw - 8;
      const u16* wp = (is1 ? W1 : W3) + (size_t)(skq * 128 + (tid >> 3) + 64 * i) * ldw + col;
      glds16v(wp, lds0 + AT_SJ + wid * 2048 + i * 1024);
    } else {
      const int e = (b * 512 + tid) * 8;
      const u16* p0 = e < D * D ? Wo + e : (e < 2 * D * D ? W2 + (e - D * D) : W4 + (e - 2 * D * D));
      int e1 = e + 2 * D * D;
      e1 = e1 < F * D ? e1 : e;
      glds16v(i == 2 ? p0 : W4 + e1, lds0 + AT_PF + wid * 1024);
    }
  };
  asm volatile("s_waitcnt vmcnt(0) lgkmcnt(0)\n\ts_barrier" : "+v"(qf), "+v"(kmx)::"memory");
  float sbound;
  {
    const u32x4 qw = __builtin_bit_cast(u32x4, qf);
    float q2 = 0.f;
#pragma unroll
    for (int i = 0; i < 4; ++i) q2 = dot2(qw[i], qw[i], q2);
    q2 = half_sum(q2);
    float k2 = max32(kmx);
    auto rr = __builtin_amdgcn_permlane32_swap(__float_as_uint(k2), __float_as_uint(k2), false, false);
    k2 = __builtin_fmaxf(__uint_as_float(rr[0]), __uint_as_float(rr[1]));
    sbound = __builtin_amdgcn_sqrtf(q2 * k2) * 1.01f;
  }

  const LDSP char* kp = (const LDSP char*)smem + AT_K + lane * 16;
  const LDSP char* lb = (const LDSP char*)smem;
  unsigned vaddr, vinc;
  if (r32 < 16) { vaddr = AT_V + hh * 256 + r32 * 16; vinc = 1024; }
  else { vaddr = AT_C + (r32 == 16 ? 0 : (r32 >= 20 && r32 < 28) ? 64 : 16); vinc = 0; }
  f32x16 o, nm, sA, sB;
  bool safe;
#pragma unroll
  for (int r = 0; r < 16; ++r) o[r] = 0.f;
#define MX3(a, b, c) __builtin_fmaxf(__builtin_fmaxf((a), (b)), (c))
#define TILEMAX(S_, OUT)                                                                                      \
  {                                                                                                           \
    float ma_ = MX3(S_[0], S_[1], S_[2]), mb_ = MX3(S_[3], S_[4], S_[5]);                                     \
    ma_ = MX3(ma_, S_[6], S_[7]); mb_ = MX3(mb_, S_[8], S_[9]);                                               \
    ma_ = MX3(ma_, S_[10], S_[11]); mb_ = MX3(mb_, S_[12], S_[13]);                                           \
    OUT = MX3(ma_, mb_, __builtin_fmaxf(S_[14], S_[15]));                                                     \
  }
  bf16x8 kn = *(const LDSP bf16x8*)(kp);
  {
    f32x16 zero;
#pragma unroll
    for (int r = 0; r < 16; ++r) zero[r] = 0.f;
    sA = MFMA32(kn, qf, zero);
    float tm;
    TILEMAX(sA, tm)
    auto rr = __builtin_amdgcn_permlane32_swap(__float_as_uint(tm), __float_as_uint(tm), false, false);
    tm = __builtin_fmaxf(__uint_as_float(rr[0]), __uint_as_float(rr[1]));
#pragma unroll
    for (int r = 0; r < 16; ++r) { nm[r] = -tm; sA[r] -= tm; }
    safe = __all(sbound - tm < 96.0f);
  }
  kn = *(const LDSP bf16x8*)(kp + 1024);
  bf16x8 va0 = *(const LDSP bf16x8*)(lb + vaddr), va1 = *(const LDSP bf16x8*)(lb + vaddr + 512);
  bf16x8 vb0, vb1;
#define EXPK(P, S_, B_)                                                                                       \
  _Pragma("unroll") for (int i = 0; i < 4; ++i)                                                               \
      P[i] = pk_bf16(__builtin_amdgcn_exp2f(S_[B_ + 2 * i]), __builtin_amdgcn_exp2f(S_[B_ + 2 * i + 1]));
#define RESCALE(SC, SN, HAVE_SN)                                                                              \
  {                                                                                                           \
    float tm_;                                                                                                \
    TILEMAX(SC, tm_)                                                                                          \
    if (__builtin_expect(__any(tm_ > ATT_THR), 0)) {                                                             \
      auto rr_ = __builtin_amdgcn_permlane32_swap(__float_as_uint(tm_), __float_as_uint(tm_), false, false);  \
      const float d_ = MX3(__uint_as_float(rr_[0]), __uint_as_float(rr_[1]), 0.f);                            \
      const float f_ = __builtin_amdgcn_exp2f(-d_);                                                           \
      _Pragma("unroll") for (int r = 0; r < 16; ++r) {                                                        \
        o[r] *= f_; SC[r] -= d_; nm[r] -= d_;                                                                 \
        if (HAVE_SN) SN[r] -= d_;                                                                             \
      }                                                                                                       \
    }                                                                                                         \
  }
#define HALF(CHK, SC, SN, VC0, VC1, VN0, VN1, TN)                                                             \
  {                                                                                                           \
    SN = MFMA32(kn, qf, nm);                                                                                  \
    asm volatile("" : "+v"(SN));                                                                              \
    kn = *(const LDSP bf16x8*)(kp + ((TN) + 1) * 1024);                                                       \
    vaddr += vinc;                                                                                            \
    VN0 = *(const LDSP bf16x8*)(lb + vaddr);                                                                  \
    VN1 = *(const LDSP bf16x8*)(lb + vaddr + 512);                                                            \
    SCHED0();                                                                                                 \
    if (CHK) RESCALE(SC, SN, true)                                                                            \
    SCHED0();                                                                                                 \
    u32x4 p0, p1;                                                                                             \
    EXPK(p0, SC, 0)                                                                                           \
    o = MFMA32(VC0, __builtin_bit_cast(bf16x8, p0), o);                                                       \
    asm volatile("" : "+v"(o));                                                                               \
    SCHED0();                                                                                                 \
    EXPK(p1, SC, 8)                                                                                           \
    o = MFMA32(VC1, __builtin_bit_cast(bf16x8, p1), o);                                                       \
    asm volatile("" : "+v"(o));                                                                               \
    SCHED0();                                                                                                 \
  }
  if (safe) {
    static_for<0, 6>([&](auto JI) {
      constexpr int j = decltype(JI)::value, t = 2 * j;
      load_kv(2 + j);
      if (j < 4) load_side(j);
      HALF(false, sA, sB, va0, va1, vb0, vb1, t + 1)
      HALF(false, sB, sA, vb0, vb1, va0, va1, t + 2)
    });
    wait_vm_bar<0>();
#pragma unroll 1
    for (int t = 12; t < 62; t += 2) {
      HALF(false, sA, sB, va0, va1, vb0, vb1, t + 1)
      HALF(false, sB, sA, vb0, vb1, va0, va1, t + 2)
    }
  } else {
#pragma unroll
    for (int g = 2; g < 8; ++g) load_kv(g);
#pragma unroll
    for (int i = 0; i < 4; ++i) load_side(i);
#pragma unroll 1
    for (int t = 0; t < 62; t += 2) {
      if (t == 12) wait_vm_bar<0>();
      HALF(true, sA, sB, va0, va1, vb0, vb1, t + 1)
      HALF(true, sB, sA, vb0, vb1, va0, va1, t + 2)
    }
  }
  {
    sB = MFMA32(kn, qf, nm);
    vaddr += vinc;
    vb0 = *(const LDSP bf16x8*)(lb + vaddr);
    vb1 = *(const LDSP bf16x8*)(lb + vaddr + 512);
    u32x4 p0, p1;
    RESCALE(sA, sB, true)
    EXPK(p0, sA, 0)
    o = MFMA32(va0, __builtin_bit_cast(bf16x8, p0), o);
    EXPK(p1, sA, 8)
    o = MFMA32(va1, __builtin_bit_cast(bf16x8, p1), o);
    RESCALE(sB, sA, false)
    EXPK(p0, sB, 0)
    o = MFMA32(vb0, __builtin_bit_cast(bf16x8, p0), o);
    EXPK(p1, sB, 8)
    o = MFMA32(vb1, __builtin_bit_cast(bf16x8, p1), o);
  }
  auto rr = __builtin_amdgcn_permlane32_swap(__float_as_uint(o[8]), __float_as_uint(o[8]), false, false);
  const float inv = 1.0f / __uint_as_float(rr[0]);
  u16* op = O + (size_t)(qt * 32 + r32) * D + head * 16 + hh * 4;
  u32x2 w0, w1;
  w0[0] = pk_bf16(o[0] * inv, o[1] * inv); w0[1] = pk_bf16(o[2] * inv, o[3] * inv);
  w1[0] = pk_bf16(o[4] * inv, o[5] * inv); w1[1] = pk_bf16(o[6] * inv, o[7] * inv);
  *(u32x2*)op = w0;
  *(u32x2*)(op + 8) = w1;

  if (b < 32 + 4 * (FP / 64)) {
    const u32x4 wc0 = *(const LDSP u32x4*)(lb + AT_SJ + wid * 2048 + lane * 16);
    const u32x4 wc1 = *(const LDSP u32x4*)(lb + AT_SJ + wid * 2048 + 1024 + lane * 16);
    float cv[8];
#pragma unroll
    for (int i = 0; i < 4; ++i) {
      cv[2 * i] = bf_lo(wc0[i]) + bf_lo(wc1[i]);
      cv[2 * i + 1] = bf_hi(wc0[i]) + bf_hi(wc1[i]);
    }
#pragma unroll
    for (int i = 0; i < 8; ++i) {
      float t = dpp_add<0x128>(cv[i]);
      auto a = __builtin_amdgcn_permlane16_swap(__float_as_uint(t), __float_as_uint(t), false, false);
      t = __uint_as_float(a[0]) + __uint_as_float(a[1]);
      auto c = __builtin_amdgcn_permlane32_swap(__float_as_uint(t), __float_as_uint(t), false, false);
      cv[i] = __uint_as_float(c[0]) + __uint_as_float(c[1]);
    }
    float* red = (float*)(smem + AT_C + 1024);
    if (lane < 8) {
      *(f32x4*)(red + wid * 64 + lane * 8) = f32x4{cv[0], cv[1], cv[2], cv[3]};
      *(f32x4*)(red + wid * 64 + lane * 8 + 4) = f32x4{cv[4], cv[5], cv[6], cv[7]};
    }
    __syncthreads();
    if (tid < 64) {
      float t = 0.f;
#pragma unroll
      for (int i = 0; i < 8; ++i) t += red[i * 64 + tid];
      const int col = scg * 64 + tid;
      if (!is1 && col >= F) t = 0.f;
      csum[skq * (D + FP) + (is1 ? 0 : D) + col] = t;
    }
  }
}

extern "C" void kernel_launch(void* const* d_in, const int* in_sizes, int n_in, void* d_out, int out_size, void* d_ws,
                              size_t ws_size, hipStream_t stream) {
  const u16* x = (const u16*)d_in[0];
  const u16* wqkv = (const u16*)d_in[1];
  const u16* wo = (const u16*)d_in[2];
  const u16* w1 = (const u16*)d_in[4];
  const u16* w2 = (const u16*)d_in[5];
  const u16* w3 = (const u16*)d_in[6];
  const u16* w4 = (const u16*)d_in[7];
  u16* out = (u16*)d_out;
  char* ws = (char*)d_ws;
  const size_t MB = 1u << 20;
  u16* q = (u16*)(ws + 0 * MB);
  u16* k = (u16*)(ws + 2 * MB);
  u16* v = (u16*)(ws + 4 * MB);
  u16* o = (u16*)(ws + 6 * MB);
  u16* h = (u16*)(ws + 8 * MB);
  float* stats = (float*)(ws + 10 * MB);
  u16* g = (u16*)(ws + 12 * MB);
  u16* x1 = (u16*)(ws + 15 * MB);
  u16* u = (u16*)(ws + 17 * MB);

  float* part = (float*)(ws + 10 * MB + 65536);
  float* kmax = (float*)(ws + 10 * MB + 32768);
  float* csum = (float*)(ws + 11 * MB);
  constexpr int RN = 8, LDSN = RN * 2 * CH;
  auto k_qkv = gemm_w<true, 3 * D>;
  auto k_wo = gemm_n<8, RN, 2, D, D, EPI_WO>;
  auto k_w1 = gemm_n<8, RN, 2, D, D, EPI_W1>;
  auto k_w2 = gemm_n<8, RN, 2, D, D, EPI_W2>;
  auto k_w3 = gemm_w<false, F>;
  auto k_w4 = gemm_n<FP / 64, RN, 2, FP, F, EPI_W4>;
  (void)hipFuncSetAttribute((const void*)k_qkv, hipFuncAttributeMaxDynamicSharedMemorySize, W_LDS);
  (void)hipFuncSetAttribute((const void*)k_wo, hipFuncAttributeMaxDynamicSharedMemorySize, LDSN);
  (void)hipFuncSetAttribute((const void*)k_w1, hipFuncAttributeMaxDynamicSharedMemorySize, LDSN);
  (void)hipFuncSetAttribute((const void*)k_w2, hipFuncAttributeMaxDynamicSharedMemorySize, LDSN);
  (void)hipFuncSetAttribute((const void*)k_w3, hipFuncAttributeMaxDynamicSharedMemorySize, W_LDS);
  (void)hipFuncSetAttribute((const void*)k_w4, hipFuncAttributeMaxDynamicSharedMemorySize, LDSN);
  (void)hipFuncSetAttribute((const void*)attn_kernel, hipFuncAttributeMaxDynamicSharedMemorySize, AT_LDS);

  k_qkv<<<16 * 12, 512, W_LDS, stream>>>(GArgs{x, wqkv, nullptr, nullptr, q, k, v, kmax, nullptr, nullptr});
  attn_kernel<<<256, 512, AT_LDS, stream>>>(q, k, v, o, w1, w3, csum, wo, w2, w4, kmax);
  k_wo<<<256, 512, LDSN, stream>>>(GArgs{o, wo, x, nullptr, h, nullptr, nullptr, nullptr, part, nullptr});
  k_w1<<<256, 512, LDSN, stream>>>(GArgs{h, w1, nullptr, nullptr, g, nullptr, nullptr, stats, part, csum});
  k_w2<<<256, 512, LDSN, stream>>>(GArgs{g, w2, h, nullptr, x1, nullptr, nullptr, stats, part, nullptr});
  k_w3<<<16 * 11, 512, W_LDS, stream>>>(GArgs{x1, w3, nullptr, nullptr, u, nullptr, nullptr, nullptr, part, csum + D});
  k_w4<<<256, 512, LDSN, stream>>>(GArgs{u, w4, h, x1, out, nullptr, nullptr, nullptr, nullptr, nullptr});
}
```

```cpp
#include <hip/hip_runtime.h>
#include <hip/hip_bf16.h>
#include <stdint.h>

typedef __attribute__((ext_vector_type(8))) short bf16x8;
typedef __attribute__((ext_vector_type(4))) short s16x4;
typedef __attribute__((ext_vector_type(16))) float f32x16;
typedef __attribute__((ext_vector_type(4))) float f32x4;
typedef __attribute__((ext_vector_type(2))) float f32x2;
typedef __attribute__((ext_vector_type(2))) __bf16 bf16x2_t;
typedef __attribute__((ext_vector_type(4))) unsigned u32x4;
typedef __attribute__((ext_vector_type(2))) unsigned u32x2;
typedef unsigned short u16;

#define DI __device__ __forceinline__
#define LDSP __attribute__((address_space(3)))
#define MFMA32(a, b, c) __builtin_amdgcn_mfma_f32_32x32x16_bf16((a), (b), (c), 0, 0, 0)
#define SCHED0() __builtin_amdgcn_sched_barrier(0)

constexpr int S = 2048, D = 512, NH = 32, F = 1376, FP = 1408;
constexpr float LN_EPS = 1e-5f;
constexpr float QSCALE = 0.25f * 1.4426950408889634f;

DI unsigned pk_bf16(float a, float b) {
  f32x2 v = {a, b};
  bf16x2_t r = __builtin_convertvector(v, bf16x2_t);
  return __builtin_bit_cast(unsigned, r);
}
DI float bf_lo(unsigned u) { return __uint_as_float(u << 16); }
DI float bf_hi(unsigned u) { return __uint_as_float(u & 0xffff0000u); }
DI float rbf(float x) { return bf_lo(pk_bf16(x, x)); }
DI float dot2(unsigned a, unsigned b, float c) {
  return __builtin_amdgcn_fdot2_f32_bf16(__builtin_bit_cast(bf16x2_t, a), __builtin_bit_cast(bf16x2_t, b), c, false);
}
template <int CTRL> DI float dpp_add(float v) {
  int r = __builtin_amdgcn_update_dpp(0, __float_as_int(v), CTRL, 0xf, 0xf, true);
  return v + __int_as_float(r);
}
template <int CTRL> DI float dpp_max(float v) {
  int r = __builtin_amdgcn_update_dpp(__float_as_int(v), __float_as_int(v), CTRL, 0xf, 0xf, false);
  return __builtin_fmaxf(v, __int_as_float(r));
}
DI float half_sum(float v) {
  auto r = __builtin_amdgcn_permlane32_swap(__float_as_uint(v), __float_as_uint(v), false, false);
  return __uint_as_float(r[0]) + __uint_as_float(r[1]);
}
DI float max32(float v) {
  v = dpp_max<0xB1>(v); v = dpp_max<0x4E>(v); v = dpp_max<0x141>(v); v = dpp_max<0x140>(v);
  auto r = __builtin_amdgcn_permlane16_swap(__float_as_uint(v), __float_as_uint(v), false, false);
  return __builtin_fmaxf(__uint_as_float(r[0]), __uint_as_float(r[1]));
}
DI float gelu_tanh(float x) {
  const float c1 = -2.0f * 0.7978845608028654f * 1.4426950408889634f;
  const float c3 = c1 * 0.044715f;
  float t = x * __builtin_fmaf(c3, x * x, c1);
  float e = __builtin_amdgcn_exp2f(t);
  return x * __builtin_amdgcn_rcpf(1.0f + e);
}

DI void glds16(unsigned voff, const void* sbase, unsigned lds_dst) {
  unsigned keep;
  asm volatile("s_mov_b32 %0, m0\n\ts_mov_b32 m0, %3\n\ts_nop 0\n\tglobal_load_lds_dwordx4 %1, %2\n\ts_mov_b32 m0, %0"
               : "=&s"(keep) : "v"(voff), "s"(sbase), "s"(lds_dst) : "memory");
}
DI void glds16v(const void* vptr, unsigned lds_dst) {
  unsigned keep;
  asm volatile("s_mov_b32 %0, m0\n\ts_mov_b32 m0, %2\n\ts_nop 0\n\tglobal_load_lds_dwordx4 %1, off\n\ts_mov_b32 m0, %0"
               : "=&s"(keep) : "v"(vptr), "s"(lds_dst) : "memory");
}
template <int N> DI void wait_vm_bar() {
  asm volatile("s_waitcnt vmcnt(%0) lgkmcnt(0)\n\ts_barrier" ::"i"(N) : "memory");
}
DI void bar_lds() { asm volatile("s_waitcnt lgkmcnt(0)\n\ts_barrier" ::: "memory"); }
template <int I, int N, class Fn> DI void static_for(Fn&& f) {
  if constexpr (I < N) {
    f(std::integral_constant<int, I>{});
    static_for<I + 1, N>(f);
  }
}
DI s16x4 tr_read(const LDSP char* p) {
  typedef short v4i16_t __attribute__((ext_vector_type(4)));
  return __builtin_bit_cast(s16x4, __builtin_amdgcn_ds_read_tr16_b64_v4i16((LDSP v4i16_t*)p));
}

constexpr int CH = 8192;
enum { EPI_WO = 1, EPI_W1 = 2, EPI_W2 = 3, EPI_W4 = 5 };

struct GArgs {
  const u16* A;
  const u16* W;
  const u16* r0;
  const u16* r1;
  u16* o0;
  u16* o1;
  u16* o2;
  float* stats;
  float* part;
  const float* csum;
};

DI unsigned b_lane_off(int lane) {
  const int hh = lane >> 5, blk = (lane >> 4) & 1, q = (lane & 15) >> 2, p = lane & 3;
  const int kr = 8 * hh + q;
  return kr * 128 + (((2 * blk + (p >> 1)) ^ (((kr >> 1) & 1) << 2)) << 4) + 8 * (p & 1);
}
DI u32x4 b_frag(const LDSP char* bp) {
  const u32x2 lo = __builtin_bit_cast(u32x2, tr_read(bp));
  const u32x2 hi = __builtin_bit_cast(u32x2, tr_read(bp + 512));
  return u32x4{lo[0], lo[1], hi[0], hi[1]};
}
DI f32x2 row8_stats(const u32x4& o) {
  float a = 0.f, q = 0.f;
#pragma unroll
  for (int i = 0; i < 4; ++i) { a = dot2(o[i], 0x3f803f80u, a); q = dot2(o[i], o[i], q); }
  a = dpp_add<0xB1>(a); q = dpp_add<0xB1>(q);
  a = dpp_add<0x4E>(a); q = dpp_add<0x4E>(q);
  a = dpp_add<0x141>(a); q = dpp_add<0x141>(q);
  return f32x2{a, q};
}

template <int NCH, int RING, int PH, int LDA, int KW, int EPI>
__global__ __launch_bounds__(512) void gemm_n(GArgs g) {
  extern __shared__ __attribute__((aligned(16))) char smem[];
  constexpr bool LN = (EPI == EPI_W1);
  constexpr bool SHIFT = (EPI == EPI_W2);
  constexpr int SLOT = 2 * CH, LDW = D;
  const int tid = threadIdx.x, lane = tid & 63, wid = __builtin_amdgcn_readfirstlane(tid >> 6);
  const int b = blockIdx.x, rb = (b & 7) * 4 + ((b >> 3) & 3), cb = b >> 5;
  const int row0 = rb * 64, col0 = cb * 64;
  const unsigned lds0 = (unsigned)(uintptr_t)smem;

  const int er = tid >> 3, ec = (tid & 7) * 8;
  const size_t eo = (size_t)(row0 + er) * D + col0 + ec;
  u32x4 rv0, rv1;
  f32x2 rst;
  f32x4 cv0, cv1;
  if constexpr (EPI == EPI_WO || EPI == EPI_W2 || EPI == EPI_W4) rv0 = *(const u32x4*)(g.r0 + eo);
  if constexpr (EPI == EPI_W4) rv1 = *(const u32x4*)(g.r1 + eo);
  if constexpr (EPI == EPI_W2) rst = *(const f32x2*)(g.stats + (row0 + er) * 2);
  if constexpr (LN) {
    rst = *(const f32x2*)(g.part + ((row0 + er) * 8 + (tid & 7)) * 2);
    const float* cp = g.csum + col0 + ec;
    cv0 = (*(const f32x4*)(cp) + *(const f32x4*)(cp + (D + FP))) +
          (*(const f32x4*)(cp + 2 * (D + FP)) + *(const f32x4*)(cp + 3 * (D + FP)));
    cv1 = (*(const f32x4*)(cp + 4) + *(const f32x4*)(cp + 4 + (D + FP))) +
          (*(const f32x4*)(cp + 4 + 2 * (D + FP)) + *(const f32x4*)(cp + 4 + 3 * (D + FP)));
  }

  const int sr = wid * 8 + (lane >> 3), sc = lane & 7;
  const unsigned aoff = (unsigned)(sr * LDA + ((sc ^ ((sr >> 1) & 7)) << 3)) * 2u;
  const unsigned boff = (unsigned)(col0 + ((sc ^ (((sr >> 1) & 1) << 2)) << 3)) * 2u;
  const char* Ab = (const char*)(g.A + (size_t)row0 * LDA);
  auto issue = [&](auto CI) {
    constexpr int c = decltype(CI)::value, slot = c % RING;
    constexpr int sh = SHIFT ? ((0x4031 >> (((c >> 1) & 3) * 4)) & 15) : 0;
    const unsigned dst = lds0 + slot * SLOT + wid * 1024;
    glds16(aoff, Ab + (size_t)sh * (LDA * 2) + c * 128, dst);
    int kr = c * 64 + sr;
    if ((c + 1) * 64 > KW) kr = kr < KW ? kr : KW - 1;
    glds16(boff + (unsigned)kr * (LDW * 2), g.W, dst + CH);
  };
  constexpr int PRE = RING < NCH ? RING : NCH;
  static_for<0, PRE>(issue);
  const int kg = wid >> 2, wm = (wid >> 1) & 1, wn = wid & 1;
  const int i32 = lane & 31, hh = lane >> 5;
  const int ar = wm * 32 + i32;
  const unsigned a_off = ar * 128 + (((4 * kg + hh) ^ ((ar >> 1) & 7)) << 4);
  const unsigned b_off = CH + kg * 4096 + (b_lane_off(lane) ^ (wn << 6));
  f32x16 acc;
#pragma unroll
  for (int r = 0; r < 16; ++r) acc[r] = 0.f;
  static_assert(RING % PH == 0 && RING >= 2 * PH && NCH % PH == 0, "ring / phase geometry");
  static_for<0, NCH>([&](auto CI) {
    constexpr int c = decltype(CI)::value, slot = c % RING;
    if constexpr (c % PH == 0) {
      constexpr int issued0 = RING + (c >= PH ? c - PH : 0);
      constexpr int issued = issued0 < NCH ? issued0 : NCH;
      wait_vm_bar<(issued - c - PH) * 2>();
      if constexpr (c >= PH)
        static_for<0, PH>([&](auto PI) {
          constexpr int cn = c - PH + RING + decltype(PI)::value;
          if constexpr (cn < NCH) issue(std::integral_constant<int, cn>{});
        });
    }
    const LDSP char* base = (const LDSP char*)smem + slot * SLOT;
#pragma unroll
    for (int s = 0; s < 2; ++s) {
      const u32x4 af = *(const LDSP u32x4*)(base + (a_off ^ (s << 5)));
      const u32x4 bf = b_frag(base + s * 2048 + b_off);
      acc = MFMA32(__builtin_bit_cast(bf16x8, bf), __builtin_bit_cast(bf16x8, af), acc);
    }
  });
  bar_lds();

  constexpr int LDS_ = 68;
  float* st = (float*)smem;
#pragma unroll
  for (int q = 0; q < 4; ++q) {
    f32x4 v = {acc[4 * q], acc[4 * q + 1], acc[4 * q + 2], acc[4 * q + 3]};
    *(f32x4*)(st + (kg * 64 + ar) * LDS_ + wn * 32 + 8 * q + 4 * hh) = v;
  }
  __syncthreads();
  float v[8];
  {
    const f32x4 a0 = *(const f32x4*)(st + er * LDS_ + ec), a1 = *(const f32x4*)(st + er * LDS_ + ec + 4);
    const f32x4 b0 = *(const f32x4*)(st + (64 + er) * LDS_ + ec), b1 = *(const f32x4*)(st + (64 + er) * LDS_ + ec + 4);
#pragma unroll
    for (int i = 0; i < 4; ++i) { v[i] = a0[i] + b0[i]; v[4 + i] = a1[i] + b1[i]; }
  }
  u32x4 o;
  if constexpr (EPI == EPI_WO) {
#pragma unroll
    for (int i = 0; i < 4; ++i) o[i] = pk_bf16(bf_lo(rv0[i]) + rbf(v[2 * i]), bf_hi(rv0[i]) + rbf(v[2 * i + 1]));
    *(u32x4*)(g.o0 + eo) = o;
    const f32x2 ps = row8_stats(o);
    if ((tid & 7) == 0) *(f32x2*)(g.part + ((row0 + er) * 8 + cb) * 2) = ps;
  } else if constexpr (EPI == EPI_W1) {
    float a = rst[0], q = rst[1];
    a = dpp_add<0xB1>(a); q = dpp_add<0xB1>(q);
    a = dpp_add<0x4E>(a); q = dpp_add<0x4E>(q);
    a = dpp_add<0x141>(a); q = dpp_add<0x141>(q);
    const float mean = a * (1.0f / D);
    const float var = fmaxf(q * (1.0f / D) - mean * mean, 0.f);
    float rs = __builtin_amdgcn_rsqf(var + LN_EPS);
    if (cb == 0 && (tid & 7) == 0) *(f32x2*)(g.stats + (row0 + er) * 2) = f32x2{mean, rs};
    rs *= __builtin_amdgcn_rsqf(var * rs * rs + LN_EPS);
    const float rm = rs * mean;
#pragma unroll
    for (int i = 0; i < 4; ++i) {
      v[i] = __builtin_fmaf(v[i], rs, -rm * cv0[i]);
      v[4 + i] = __builtin_fmaf(v[4 + i], rs, -rm * cv1[i]);
    }
#pragma unroll
    for (int i = 0; i < 4; ++i) o[i] = pk_bf16(gelu_tanh(v[2 * i]), gelu_tanh(v[2 * i + 1]));
    *(u32x4*)(g.o0 + eo + 2 * D) = o;
    if ((rb == 0 || rb == 31) && tid < 16) {
      const int hr = (rb == 0 ? 0 : S + 2) + (tid >> 3);
      *(u32x4*)(g.o0 + (size_t)hr * D + col0 + ec) = u32x4{0u, 0u, 0u, 0u};
    }
  } else if constexpr (EPI == EPI_W2) {
#pragma unroll
    for (int i = 0; i < 4; ++i) {
      const float xl = rbf((bf_lo(rv0[i]) - rst[0]) * rst[1]), xh = rbf((bf_hi(rv0[i]) - rst[0]) * rst[1]);
      o[i] = pk_bf16(xl + rbf(v[2 * i]), xh + rbf(v[2 * i + 1]));
    }
    *(u32x4*)(g.o0 + eo) = o;
    const f32x2 ps = row8_stats(o);
    if ((tid & 7) == 0) *(f32x2*)(g.part + ((row0 + er) * 8 + cb) * 2) = ps;
  } else {
#pragma unroll
    for (int i = 0; i < 4; ++i)
      o[i] = pk_bf16(bf_lo(rv0[i]) + rbf(bf_lo(rv1[i]) + rbf(v[2 * i])),
                     bf_hi(rv0[i]) + rbf(bf_hi(rv1[i]) + rbf(v[2 * i + 1])));
    *(u32x4*)(g.o0 + eo) = o;
  }
}

constexpr int W_RING = 4, W_SLOT = 4 * CH, W_TAB = W_RING * W_SLOT;
constexpr int W_LDS = W_TAB + 16384 + 8192;

template <bool QKV, int LDW>
__global__ __launch_bounds__(512) void gemm_w(GArgs g) {
  extern __shared__ __attribute__((aligned(16))) char smem[];
  constexpr int NCH = 8, RING = W_RING, PH = 2, SLOT = W_SLOT;
  const int tid = threadIdx.x, lane = tid & 63, wid = __builtin_amdgcn_readfirstlane(tid >> 6);
  const int b = blockIdx.x, rb = (b & 7) * 2 + ((b >> 3) & 1), cb = b >> 4;
  const int row0 = rb * 128, col0 = cb * 128;
  const unsigned lds0 = (unsigned)(uintptr_t)smem;
  float* rowt = (float*)(smem + W_TAB);
  float* colt = rowt + 256;
  float* rowp = colt + 128;
  float* colp = rowp + 2048;

  f32x4 pv[4];
  float cvv[4];
  if constexpr (!QKV) {
    const float* pp = g.part + (row0 + (tid & 127)) * 16;
    const float* cp = g.csum + col0 + (tid & 127);
#pragma unroll
    for (int i = 0; i < 4; ++i)
      asm volatile("global_load_dwordx4 %0, %1, off offset:%2" : "=v"(pv[i]) : "v"(pp), "i"(16 * i) : "memory");
#pragma unroll
    for (int i = 0; i < 4; ++i)
      asm volatile("global_load_dword %0, %1, off" : "=v"(cvv[i]) : "v"(cp + i * (D + FP)) : "memory");
  }
  const int sr = wid * 8 + (lane >> 3), sc = lane & 7;
  const unsigned aoff = (unsigned)(sr * D + ((sc ^ ((sr >> 1) & 7)) << 3)) * 2u;
  unsigned boff[2];
#pragma unroll
  for (int st = 0; st < 2; ++st) {
    int col = col0 + st * 64 + ((sc ^ (((sr >> 1) & 1) << 2)) << 3);
    if (!QKV) col = col < LDW - 8 ? col : LDW - 8;
    boff[st] = (unsigned)(sr * LDW + col) * 2u;
  }
  const char* Ab = (const char*)(g.A + (size_t)row0 * D);
  auto issue = [&](auto CI) {
    constexpr int c = decltype(CI)::value, slot = c % RING;
    const unsigned dst = lds0 + slot * SLOT + wid * 1024;
    glds16(aoff, Ab + c * 128, dst);
    glds16(aoff, Ab + 64 * D * 2 + c * 128, dst + CH);
    const char* wb = (const char*)g.W + (size_t)c * 64 * LDW * 2;
    glds16(boff[0], wb, dst + 2 * CH);
    glds16(boff[1], wb, dst + 3 * CH);
  };
  static_for<0, RING>(issue);
  if constexpr (QKV) {
    unsigned long long pc;
    int d0, d1, d2, d3, d4, d5;
    asm volatile(
        "s_getpc_b64 %0\n.Lcode_pf_anchor:\n\t"
        "s_mov_b32 %1, _Z11attn_kernelPKtS0_S0_PtS0_S0_PfS0_S0_S0_PKf-.Lcode_pf_anchor\n\t"
        "s_mov_b32 %2, _Z6gemm_nILi8ELi8ELi2ELi512ELi512ELi1EEv5GArgs-.Lcode_pf_anchor\n\t"
        "s_mov_b32 %3, _Z6gemm_nILi8ELi8ELi2ELi512ELi512ELi2EEv5GArgs-.Lcode_pf_anchor\n\t"
        "s_mov_b32 %4, _Z6gemm_nILi8ELi8ELi2ELi512ELi512ELi3EEv5GArgs-.Lcode_pf_anchor\n\t"
        "s_mov_b32 %5, _Z6gemm_wILb0ELi1376EEv5GArgs-.Lcode_pf_anchor\n\t"
        "s_mov_b32 %6, _Z6gemm_nILi22ELi8ELi2ELi1408ELi1376ELi5EEv5GArgs-.Lcode_pf_anchor"
        : "=s"(pc), "=s"(d0), "=s"(d1), "=s"(d2), "=s"(d3), "=s"(d4), "=s"(d5));
    const int j = (b >> 3) * 8 + wid;
    int dl = d0 + min(j * 1024, 8704 - 1024);
    if (j >= 9) dl = d1 + min((j - 9) * 1024, 2304 - 1024);
    if (j >= 12) dl = d2 + min((j - 12) * 1024, 2816 - 1024);
    if (j >= 15) dl = d3 + min((j - 15) * 1024, 2560 - 1024);
    if (j >= 18) dl = d4 + min((j - 18) * 1024, 5888 - 1024);
    if (j >= 24) dl = d5 + (j - 24) * 1024;
    if (j < 28) glds16(lane * 16, (const char*)pc + dl, lds0 + W_LDS - 8192 + wid * 1024);
  }

  if constexpr (!QKV) {
    asm volatile("s_waitcnt vmcnt(16)" : "+v"(pv[0]), "+v"(pv[1]), "+v"(pv[2]), "+v"(pv[3]),
                 "+v"(cvv[0]), "+v"(cvv[1]), "+v"(cvv[2]), "+v"(cvv[3])::"memory");
    if (tid < 128) {
      const float a = (pv[0][0] + pv[0][2]) + (pv[1][0] + pv[1][2]) + (pv[2][0] + pv[2][2]) + (pv[3][0] + pv[3][2]);
      const float q = (pv[0][1] + pv[0][3]) + (pv[1][1] + pv[1][3]) + (pv[2][1] + pv[2][3]) + (pv[3][1] + pv[3][3]);
      const float mean = a * (1.0f / D);
      const float rs = __builtin_amdgcn_rsqf(fmaxf(q * (1.0f / D) - mean * mean, 0.f) + LN_EPS);
      *(f32x2*)(rowt + tid * 2) = f32x2{rs, rs * mean};
    } else if (tid < 256) {
      colt[tid - 128] = (cvv[0] + cvv[1]) + (cvv[2] + cvv[3]);
    }
  }

  const int wm = wid >> 2, wn = wid & 3;
  const int i32 = lane & 31, hh = lane >> 5;
  const int ar = wm * 64 + i32;
  const unsigned a_off = ar * 128 + ((hh ^ ((ar >> 1) & 7)) << 4);
  const unsigned b_off = 2 * CH + (wn >> 1) * CH + (b_lane_off(lane) ^ ((wn & 1) << 6));
  f32x16 acc[2];
  float s1[2][2], s2[2][2], cs[2];
#pragma unroll
  for (int i = 0; i < 2; ++i) {
    s1[i][0] = s1[i][1] = s2[i][0] = s2[i][1] = cs[i] = 0.f;
#pragma unroll
    for (int r = 0; r < 16; ++r) acc[i][r] = 0.f;
  }
  auto mainloop = [&](auto VO) {
    constexpr bool VOR = decltype(VO)::value;
    static_for<0, NCH>([&](auto CI) {
      constexpr int c = decltype(CI)::value, slot = c % RING;
      if constexpr (c % PH == 0) {
        constexpr int issued0 = RING + (c >= PH ? c - PH : 0);
        constexpr int issued = issued0 < NCH ? issued0 : NCH;
        wait_vm_bar<(issued - c - PH) * 4>();
        if constexpr (c >= PH)
          static_for<0, PH>([&](auto PI) {
            constexpr int cn = c - PH + RING + decltype(PI)::value;
            if constexpr (cn < NCH) issue(std::integral_constant<int, cn>{});
          });
      }
      const LDSP char* base = (const LDSP char*)smem + slot * SLOT;
#pragma unroll
      for (int s = 0; s < 4; ++s) {
        const u32x4 a0 = *(const LDSP u32x4*)(base + (a_off ^ (s << 5)));
        const u32x4 a1 = *(const LDSP u32x4*)(base + 4096 + (a_off ^ (s << 5)));
        const u32x4 bf = b_frag(base + s * 2048 + b_off);
        if constexpr (VOR) {
          acc[0] = MFMA32(__builtin_bit_cast(bf16x8, a0), __builtin_bit_cast(bf16x8, bf), acc[0]);
          acc[1] = MFMA32(__builtin_bit_cast(bf16x8, a1), __builtin_bit_cast(bf16x8, bf), acc[1]);
        } else {
          acc[0] = MFMA32(__builtin_bit_cast(bf16x8, bf), __builtin_bit_cast(bf16x8, a0), acc[0]);
          acc[1] = MFMA32(__builtin_bit_cast(bf16x8, bf), __builtin_bit_cast(bf16x8, a1), acc[1]);
        }
      }
      if constexpr (QKV) {
#pragma unroll
        for (int i = 0; i < 2; ++i) {
          const u32x4 a = *(const LDSP u32x4*)(base + i * 4096 + (a_off ^ (wn << 5)));
#pragma unroll
          for (int q = 0; q < 4; ++q) {
            s1[i][q & 1] = dot2(a[q], 0x3f803f80u, s1[i][q & 1]);
            s2[i][q & 1] = dot2(a[q], a[q], s2[i][q & 1]);
          }
          const u32x4 bb = b_frag(base + (2 * wm + i) * 2048 + b_off);
#pragma unroll
          for (int q = 0; q < 4; ++q) cs[i] = dot2(bb[q], 0x3f803f80u, cs[i]);
          asm volatile("" : "+v"(s1[i][0]), "+v"(s1[i][1]), "+v"(s2[i][0]), "+v"(s2[i][1]), "+v"(cs[i]));
        }
      }
    });
  };
  const bool isv = QKV && col0 >= 2 * D;
  if (isv) mainloop(std::true_type{});
  else mainloop(std::false_type{});

  if constexpr (QKV) {
#pragma unroll
    for (int i = 0; i < 2; ++i)
      *(f32x2*)(rowp + ((wn * 2 + hh) * 128 + ar + 32 * i) * 2) = f32x2{s1[i][0] + s1[i][1], s2[i][0] + s2[i][1]};
    colp[(wm * 2 + hh) * 128 + wn * 32 + i32] = cs[0] + cs[1];
    __syncthreads();
    if (tid < 128) {
      float a = 0.f, q = 0.f;
#pragma unroll
      for (int p = 0; p < 8; ++p) { f32x2 v = *(const f32x2*)(rowp + (p * 128 + tid) * 2); a += v[0]; q += v[1]; }
      const float mean = a * (1.0f / D);
      const float rs = __builtin_amdgcn_rsqf(fmaxf(q * (1.0f / D) - mean * mean, 0.f) + LN_EPS);
      *(f32x2*)(rowt + tid * 2) = f32x2{rs, rs * mean};
    } else if (tid < 256) {
      const int n = tid - 128;
      colt[n] = colp[n] + colp[128 + n] + colp[256 + n] + colp[384 + n];
    }
    __syncthreads();
    if (isv) {
      const float cn = colt[wn * 32 + i32];
      const int vc = col0 - 2 * D + wn * 32 + i32;
#pragma unroll
      for (int i = 0; i < 2; ++i) {
        u16* dst = g.o2 + (size_t)(vc >> 4) * 32768 + ((row0 >> 5) + wm * 2 + i) * 512 + hh * 128 + (vc & 15) * 8;
#pragma unroll
        for (int s = 0; s < 2; ++s) {
          u32x4 w;
#pragma unroll
          for (int e = 0; e < 4; ++e) {
            const int r0_ = 8 * s + 2 * e;
            const int m = wm * 64 + i * 32 + (r0_ & 3) + 8 * (r0_ >> 2) + 4 * hh;
            const f32x4 rt = *(const f32x4*)(rowt + m * 2);
            w[e] = pk_bf16(__builtin_fmaf(acc[i][r0_], rt[0], -rt[1] * cn), __builtin_fmaf(acc[i][r0_ + 1], rt[2], -rt[3] * cn));
          }
          *(u32x4*)(dst + s * 256) = w;
        }
      }
    } else {
      const float sc_ = col0 < D ? QSCALE : 1.0f;
      u16* dbase = (col0 < D ? g.o0 : g.o1) + (size_t)(((col0 & (D - 1)) + wn * 32) >> 4) * 32768 + i32 * 8 + hh * 4;
#pragma unroll
      for (int i = 0; i < 2; ++i) {
        const f32x2 rt = *(const f32x2*)(rowt + (ar + 32 * i) * 2);
        const float rs = rt[0] * sc_, rm = rt[1] * sc_;
        u16* dst = dbase + ((row0 >> 5) + wm * 2 + i) * 512;
        float ss[2] = {0.f, 0.f};
#pragma unroll
        for (int q = 0; q < 4; ++q) {
          const f32x4 cn = *(const f32x4*)(colt + wn * 32 + 8 * q + 4 * hh);
          u32x2 w;
          w[0] = pk_bf16(__builtin_fmaf(acc[i][4 * q], rs, -rm * cn[0]), __builtin_fmaf(acc[i][4 * q + 1], rs, -rm * cn[1]));
          w[1] = pk_bf16(__builtin_fmaf(acc[i][4 * q + 2], rs, -rm * cn[2]), __builtin_fmaf(acc[i][4 * q + 3], rs, -rm * cn[3]));
          *(u32x2*)(dst + (q >> 1) * 32768 + (q & 1) * 256) = w;
          ss[q >> 1] = dot2(w[0], w[0], dot2(w[1], w[1], ss[q >> 1]));
        }
        if (col0 >= D) {
          const float m0 = max32(half_sum(ss[0])), m1 = max32(half_sum(ss[1]));
          if (lane == 0) {
            float* km = g.stats + (((col0 - D) + wn * 32) >> 4) * 64 + (row0 >> 5) + wm * 2 + i;
            km[0] = m0;
            km[64] = m1;
          }
        }
      }
    }
  } else {
    bar_lds();
    u16* st = (u16*)smem;
#pragma unroll
    for (int i = 0; i < 2; ++i) {
      const f32x2 rt = *(const f32x2*)(rowt + (ar + 32 * i) * 2);
#pragma unroll
      for (int q = 0; q < 4; ++q) {
        const f32x4 cn = *(const f32x4*)(colt + wn * 32 + 8 * q + 4 * hh);
        float t[4];
#pragma unroll
        for (int e = 0; e < 4; ++e) t[e] = gelu_tanh(__builtin_fmaf(acc[i][4 * q + e], rt[0], -rt[1] * cn[e]));
        *(u32x2*)(st + (ar + 32 * i) * 136 + wn * 32 + 8 * q + 4 * hh) = u32x2{pk_bf16(t[0], t[1]), pk_bf16(t[2], t[3])};
      }
    }
    __syncthreads();
#pragma unroll
    for (int it = 0; it < 4; ++it) {
      const int item = it * 512 + tid, r = item >> 4, c = (item & 15) * 8;
      u32x4 o = *(const u32x4*)(st + r * 136 + c);
      if (col0 + c >= F) o = u32x4{0u, 0u, 0u, 0u};
      *(u32x4*)(g.o0 + (size_t)(row0 + r) * FP + col0 + c) = o;
    }
  }
}

#define ATT_THR 8.0f
constexpr int AT_K = 0, AT_V = 65536, AT_C = 131072, AT_PF = AT_C + 1024 + 2048, AT_SJ = AT_PF + 8192,
              AT_LDS = AT_SJ + 16384;

__global__ __launch_bounds__(512) void attn_kernel(const u16* __restrict__ Qf, const u16* __restrict__ Kf,
                                                   const u16* __restrict__ Vf, u16* __restrict__ O,
                                                   const u16* __restrict__ W1, const u16* __restrict__ W3,
                                                   float* __restrict__ csum, const u16* __restrict__ Wo,
                                                   const u16* __restrict__ W2, const u16* __restrict__ W4,
                                                   const float* __restrict__ kmax) {
  extern __shared__ __attribute__((aligned(16))) char smem[];
  const int tid = threadIdx.x, lane = tid & 63, wid = __builtin_amdgcn_readfirstlane(tid >> 6);
  const int b = blockIdx.x, head = (b & 7) * 4 + ((b >> 3) & 3), qb = b >> 5;
  const unsigned lds0 = (unsigned)(uintptr_t)smem;
  const int r32 = lane & 31, hh = lane >> 5;
  const int qt = qb * 8 + wid;

  bf16x8 qf;
  {
    const u16* qp = Qf + (size_t)head * 32768 + qt * 512 + lane * 8;
    asm volatile("global_load_dwordx4 %0, %1, off" : "=v"(qf) : "v"(qp) : "memory");
  }
  float kmx;
  asm volatile("global_load_dword %0, %1, off" : "=v"(kmx) : "v"(kmax + head * 64 + lane) : "memory");
  const char* kb = (const char*)(Kf + (size_t)head * 32768) + wid * 1024;
  const char* vb = (const char*)(Vf + (size_t)head * 32768) + wid * 1024;
  auto load_kv = [&](int g) {
    glds16(lane * 16, kb + g * 8192, lds0 + AT_K + g * 8192 + wid * 1024);
    glds16(lane * 16, vb + g * 8192, lds0 + AT_V + g * 8192 + wid * 1024);
  };
  load_kv(0);
  load_kv(1);
  if (tid < 32) {
    const unsigned w = ((tid & 15) == 0) ? 0x3f803f80u : 0u;
    *(LDSP u32x4*)((LDSP char*)smem + AT_C + (tid >> 4) * 512 + (tid & 15) * 16) = u32x4{w, w, w, w};
  }
  const bool is1 = b < 32;
  const int sj = is1 ? b : b - 32, scg = sj >> 2, skq = sj & 3;
  auto load_side = [&](int i) {
    if (i < 2) {
      const int ldw = is1 ? D : F;
      int col = scg * 64 + (tid & 7) * 8;
      col = col < ldw - 8 ? col : ldw - 8;
      const u16* wp = (is1 ? W1 : W3) + (size_t)(skq * 128 + (tid >> 3) + 64 * i) * ldw + col;
      glds16v(wp, lds0 + AT_SJ + wid * 2048 + i * 1024);
    } else {
      const int e = (b * 512 + tid) * 8;
      const u16* p0 = e < D * D ? Wo + e : (e < 2 * D * D ? W2 + (e - D * D) : W4 + (e - 2 * D * D));
      int e1 = e + 2 * D * D;
      e1 = e1 < F * D ? e1 : e;
      glds16v(i == 2 ? p0 : W4 + e1, lds0 + AT_PF + wid * 1024);
    }
  };
  asm volatile("s_waitcnt vmcnt(0) lgkmcnt(0)\n\ts_barrier" : "+v"(qf), "+v"(kmx)::"memory");
  float sbound;
  {
    const u32x4 qw = __builtin_bit_cast(u32x4, qf);
    float q2 = 0.f;
#pragma unroll
    for (int i = 0; i < 4; ++i) q2 = dot2(qw[i], qw[i], q2);
    q2 = half_sum(q2);
    float k2 = max32(kmx);
    auto rr = __builtin_amdgcn_permlane32_swap(__float_as_uint(k2), __float_as_uint(k2), false, false);
    k2 = __builtin_fmaxf(__uint_as_float(rr[0]), __uint_as_float(rr[1]));
    sbound = __builtin_amdgcn_sqrtf(q2 * k2) * 1.01f;
  }

  const LDSP char* kp = (const LDSP char*)smem + AT_K + lane * 16;
  const LDSP char* lb = (const LDSP char*)smem;
  unsigned vaddr, vinc;
  if (r32 < 16) { vaddr = AT_V + hh * 256 + r32 * 16; vinc = 1024; }
  else { vaddr = AT_C + (r32 == 16 ? 0 : (r32 >= 20 && r32 < 28) ? 64 : 16); vinc = 0; }
  f32x16 o, nm, sA, sB;
  bool safe;
#pragma unroll
  for (int r = 0; r < 16; ++r) o[r] = 0.f;
#define MX3(a, b, c) __builtin_fmaxf(__builtin_fmaxf((a), (b)), (c))
#define TILEMAX(S_, OUT)                                                                                      \
  {                                                                                                           \
    float ma_ = MX3(S_[0], S_[1], S_[2]), mb_ = MX3(S_[3], S_[4], S_[5]);                                     \
    ma_ = MX3(ma_, S_[6], S_[7]); mb_ = MX3(mb_, S_[8], S_[9]);                                               \
    ma_ = MX3(ma_, S_[10], S_[11]); mb_ = MX3(mb_, S_[12], S_[13]);                                           \
    OUT = MX3(ma_, mb_, __builtin_fmaxf(S_[14], S_[15]));                                                     \
  }
  bf16x8 kn = *(const LDSP bf16x8*)(kp);
  {
    f32x16 zero;
#pragma unroll
    for (int r = 0; r < 16; ++r) zero[r] = 0.f;
    sA = MFMA32(kn, qf, zero);
    float tm;
    TILEMAX(sA, tm)
    auto rr = __builtin_amdgcn_permlane32_swap(__float_as_uint(tm), __float_as_uint(tm), false, false);
    tm = __builtin_fmaxf(__uint_as_float(rr[0]), __uint_as_float(rr[1]));
#pragma unroll
    for (int r = 0; r < 16; ++r) { nm[r] = -tm; sA[r] -= tm; }
    safe = __all(sbound - tm < 96.0f);
  }
  kn = *(const LDSP bf16x8*)(kp + 1024);
  bf16x8 va0 = *(const LDSP bf16x8*)(lb + vaddr), va1 = *(const LDSP bf16x8*)(lb + vaddr + 512);
  bf16x8 vb0, vb1;
#define EXPK(P, S_, B_)                                                                                       \
  _Pragma("unroll") for (int i = 0; i < 4; ++i)                                                               \
      P[i] = pk_bf16(__builtin_amdgcn_exp2f(S_[B_ + 2 * i]), __builtin_amdgcn_exp2f(S_[B_ + 2 * i + 1]));
#define RESCALE(SC, SN, HAVE_SN)                                                                              \
  {                                                                                                           \
    float tm_;                                                                                                \
    TILEMAX(SC, tm_)                                                                                          \
    if (__builtin_expect(__any(tm_ > ATT_THR), 0)) {                                                             \
      auto rr_ = __builtin_amdgcn_permlane32_swap(__float_as_uint(tm_), __float_as_uint(tm_), false, false);  \
      const float d_ = MX3(__uint_as_float(rr_[0]), __uint_as_float(rr_[1]), 0.f);                            \
      const float f_ = __builtin_amdgcn_exp2f(-d_);                                                           \
      _Pragma("unroll") for (int r = 0; r < 16; ++r) {                                                        \
        o[r] *= f_; SC[r] -= d_; nm[r] -= d_;                                                                 \
        if (HAVE_SN) SN[r] -= d_;                                                                             \
      }                                                                                                       \
    }                                                                                                         \
  }
#define HALF(CHK, SC, SN, VC0, VC1, VN0, VN1, TN)                                                             \
  {                                                                                                           \
    SN = MFMA32(kn, qf, nm);                                                                                  \
    asm volatile("" : "+v"(SN));                                                                              \
    kn = *(const LDSP bf16x8*)(kp + ((TN) + 1) * 1024);                                                       \
    vaddr += vinc;                                                                                            \
    VN0 = *(const LDSP bf16x8*)(lb + vaddr);                                                                  \
    VN1 = *(const LDSP bf16x8*)(lb + vaddr + 512);                                                            \
    SCHED0();                                                                                                 \
    if (CHK) RESCALE(SC, SN, true)                                                                            \
    SCHED0();                                                                                                 \
    u32x4 p0, p1;                                                                                             \
    EXPK(p0, SC, 0)                                                                                           \
    o = MFMA32(VC0, __builtin_bit_cast(bf16x8, p0), o);                                                       \
    asm volatile("" : "+v"(o));                                                                               \
    SCHED0();                                                                                                 \
    EXPK(p1, SC, 8)                                                                                           \
    o = MFMA32(VC1, __builtin_bit_cast(bf16x8, p1), o);                                                       \
    asm volatile("" : "+v"(o));                                                                               \
    SCHED0();                                                                                                 \
  }
  if (safe) {
    static_for<0, 6>([&](auto JI) {
      constexpr int j = decltype(JI)::value, t = 2 * j;
      load_kv(2 + j);
      if (j < 4) load_side(j);
      HALF(false, sA, sB, va0, va1, vb0, vb1, t + 1)
      HALF(false, sB, sA, vb0, vb1, va0, va1, t + 2)
    });
    wait_vm_bar<0>();
#pragma unroll 1
    for (int t = 12; t < 62; t += 2) {
      HALF(false, sA, sB, va0, va1, vb0, vb1, t + 1)
      HALF(false, sB, sA, vb0, vb1, va0, va1, t + 2)
    }
  } else {
#pragma unroll
    for (int g = 2; g < 8; ++g) load_kv(g);
#pragma unroll
    for (int i = 0; i < 4; ++i) load_side(i);
#pragma unroll 1
    for (int t = 0; t < 62; t += 2) {
      if (t == 12) wait_vm_bar<0>();
      HALF(true, sA, sB, va0, va1, vb0, vb1, t + 1)
      HALF(true, sB, sA, vb0, vb1, va0, va1, t + 2)
    }
  }
  {
    sB = MFMA32(kn, qf, nm);
    vaddr += vinc;
    vb0 = *(const LDSP bf16x8*)(lb + vaddr);
    vb1 = *(const LDSP bf16x8*)(lb + vaddr + 512);
    u32x4 p0, p1;
    RESCALE(sA, sB, true)
    EXPK(p0, sA, 0)
    o = MFMA32(va0, __builtin_bit_cast(bf16x8, p0), o);
    EXPK(p1, sA, 8)
    o = MFMA32(va1, __builtin_bit_cast(bf16x8, p1), o);
    RESCALE(sB, sA, false)
    EXPK(p0, sB, 0)
    o = MFMA32(vb0, __builtin_bit_cast(bf16x8, p0), o);
    EXPK(p1, sB, 8)
    o = MFMA32(vb1, __builtin_bit_cast(bf16x8, p1), o);
  }
  auto rr = __builtin_amdgcn_permlane32_swap(__float_as_uint(o[8]), __float_as_uint(o[8]), false, false);
  const float inv = 1.0f / __uint_as_float(rr[0]);
  u16* op = O + (size_t)(qt * 32 + r32) * D + head * 16 + hh * 4;
  u32x2 w0, w1;
  w0[0] = pk_bf16(o[0] * inv, o[1] * inv); w0[1] = pk_bf16(o[2] * inv, o[3] * inv);
  w1[0] = pk_bf16(o[4] * inv, o[5] * inv); w1[1] = pk_bf16(o[6] * inv, o[7] * inv);
  *(u32x2*)op = w0;
  *(u32x2*)(op + 8) = w1;

  if (b < 32 + 4 * (FP / 64)) {
    const u32x4 wc0 = *(const LDSP u32x4*)(lb + AT_SJ + wid * 2048 + lane * 16);
    const u32x4 wc1 = *(const LDSP u32x4*)(lb + AT_SJ + wid * 2048 + 1024 + lane * 16);
    float cv[8];
#pragma unroll
    for (int i = 0; i < 4; ++i) {
      cv[2 * i] = bf_lo(wc0[i]) + bf_lo(wc1[i]);
      cv[2 * i + 1] = bf_hi(wc0[i]) + bf_hi(wc1[i]);
    }
#pragma unroll
    for (int i = 0; i < 8; ++i) {
      float t = dpp_add<0x128>(cv[i]);
      auto a = __builtin_amdgcn_permlane16_swap(__float_as_uint(t), __float_as_uint(t), false, false);
      t = __uint_as_float(a[0]) + __uint_as_float(a[1]);
      auto c = __builtin_amdgcn_permlane32_swap(__float_as_uint(t), __float_as_uint(t), false, false);
      cv[i] = __uint_as_float(c[0]) + __uint_as_float(c[1]);
    }
    float* red = (float*)(smem + AT_C + 1024);
    if (lane < 8) {
      *(f32x4*)(red + wid * 64 + lane * 8) = f32x4{cv[0], cv[1], cv[2], cv[3]};
      *(f32x4*)(red + wid * 64 + lane * 8 + 4) = f32x4{cv[4], cv[5], cv[6], cv[7]};
    }
    __syncthreads();
    if (tid < 64) {
      float t = 0.f;
#pragma unroll
      for (int i = 0; i < 8; ++i) t += red[i * 64 + tid];
      const int col = scg * 64 + tid;
      if (!is1 && col >= F) t = 0.f;
      csum[skq * (D + FP) + (is1 ? 0 : D) + col] = t;
    }
  }
}

extern "C" void kernel_launch(void* const* d_in, const int* in_sizes, int n_in, void* d_out, int out_size, void* d_ws,
                              size_t ws_size, hipStream_t stream) {
  const u16* x = (const u16*)d_in[0];
  const u16* wqkv = (const u16*)d_in[1];
  const u16* wo = (const u16*)d_in[2];
  const u16* w1 = (const u16*)d_in[4];
  const u16* w2 = (const u16*)d_in[5];
  const u16* w3 = (const u16*)d_in[6];
  const u16* w4 = (const u16*)d_in[7];
  u16* out = (u16*)d_out;
  char* ws = (char*)d_ws;
  const size_t MB = 1u << 20;
  u16* q = (u16*)(ws + 0 * MB);
  u16* k = (u16*)(ws + 2 * MB);
  u16* v = (u16*)(ws + 4 * MB);
  u16* o = (u16*)(ws + 6 * MB);
  u16* h = (u16*)(ws + 8 * MB);
  float* stats = (float*)(ws + 10 * MB);
  u16* g = (u16*)(ws + 12 * MB);
  u16* x1 = (u16*)(ws + 15 * MB);
  u16* u = (u16*)(ws + 17 * MB);

  float* part = (float*)(ws + 10 * MB + 65536);
  float* kmax = (float*)(ws + 10 * MB + 32768);
  float* csum = (float*)(ws + 11 * MB);
  constexpr int RN = 8, LDSN = RN * 2 * CH;
  auto k_qkv = gemm_w<true, 3 * D>;
  auto k_wo = gemm_n<8, RN, 2, D, D, EPI_WO>;
  auto k_w1 = gemm_n<8, RN, 2, D, D, EPI_W1>;
  auto k_w2 = gemm_n<8, RN, 2, D, D, EPI_W2>;
  auto k_w3 = gemm_w<false, F>;
  auto k_w4 = gemm_n<FP / 64, RN, 2, FP, F, EPI_W4>;
  (void)hipFuncSetAttribute((const void*)k_qkv, hipFuncAttributeMaxDynamicSharedMemorySize, W_LDS);
  (void)hipFuncSetAttribute((const void*)k_wo, hipFuncAttributeMaxDynamicSharedMemorySize, LDSN);
  (void)hipFuncSetAttribute((const void*)k_w1, hipFuncAttributeMaxDynamicSharedMemorySize, LDSN);
  (void)hipFuncSetAttribute((const void*)k_w2, hipFuncAttributeMaxDynamicSharedMemorySize, LDSN);
  (void)hipFuncSetAttribute((const void*)k_w3, hipFuncAttributeMaxDynamicSharedMemorySize, W_LDS);
  (void)hipFuncSetAttribute((const void*)k_w4, hipFuncAttributeMaxDynamicSharedMemorySize, LDSN);
  (void)hipFuncSetAttribute((const void*)attn_kernel, hipFuncAttributeMaxDynamicSharedMemorySize, AT_LDS);

  k_qkv<<<16 * 12, 512, W_LDS, stream>>>(GArgs{x, wqkv, nullptr, nullptr, q, k, v, kmax, nullptr, nullptr});
  attn_kernel<<<256, 512, AT_LDS, stream>>>(q, k, v, o, w1, w3, csum, wo, w2, w4, kmax);
  k_wo<<<256, 512, LDSN, stream>>>(GArgs{o, wo, x, nullptr, h, nullptr, nullptr, nullptr, part, nullptr});
  k_w1<<<256, 512, LDSN, stream>>>(GArgs{h, w1, nullptr, nullptr, g, nullptr, nullptr, stats, part, csum});
  k_w2<<<256, 512, LDSN, stream>>>(GArgs{g, w2, h, nullptr, x1, nullptr, nullptr, stats, part, nullptr});
  k_w3<<<16 * 11, 512, W_LDS, stream>>>(GArgs{x1, w3, nullptr, nullptr, u, nullptr, nullptr, nullptr, part, csum + D});
  k_w4<<<256, 512, LDSN, stream>>>(GArgs{u, w4, h, x1, out, nullptr, nullptr, nullptr, nullptr, nullptr});
}
```
